```python
import torch

def _softmax(x, axis=-1):
    return torch.softmax(x, dim=axis)
def _gelu(x):
    return torch.nn.functional.gelu(x, approximate="tanh")
def _silu(x):
    return torch.nn.functional.silu(x)
def _sigmoid(x):
    return torch.sigmoid(x)
def _layernorm(x, axis=-1, eps=1e-5):
    m = x.mean(dim=axis, keepdim=True)
    v = x.var(dim=axis, keepdim=True, unbiased=False)
    return (x - m) / torch.sqrt(v + eps)
def _scatter_add(tgt, idx, src, axis=0):
    return tgt.clone().index_add_(axis, idx, src)
def _segment_sum(src, seg, n):
    out = torch.zeros((n,) + src.shape[1:], dtype=src.dtype)
    return out.index_add_(0, seg, src)
def _pad(x, pw):
    flat = tuple(v for pair in reversed(pw) for v in pair)
    return torch.nn.functional.pad(x, flat)

kernel_name = 'sequential_preLN__mha_block_cached__swiglu_ffn__decode'

def setup_inputs():

    _w = (lambda a: (lambda t: t.to(torch.bfloat16) if t.is_floating_point() else t)(torch.as_tensor(a)))
    def _A_setup():
        B, Sq, D = 1, 1, 640
        H, Sk, Dh = 32, 16384, 128
        return {
            "x": _w((lambda *s: torch.randn(*s, dtype=torch.bfloat16) * 0.05)(B, Sq, D)),
            "k_cache": _w((lambda *s: torch.randn(*s, dtype=torch.bfloat16) * 3.2)(B, H, Sk, Dh)),
            "v_cache": _w((lambda *s: torch.randn(*s, dtype=torch.bfloat16) * 0.4)(B, H, Sk, Dh)),
            "wq": _w((lambda *s: torch.randn(*s, dtype=torch.bfloat16) * 3.2)(D, H * Dh) * (D ** -0.5)),
            "wo": _w((lambda *s: torch.randn(*s, dtype=torch.bfloat16) * 0.4)(H * Dh, D) * ((H * Dh) ** -0.5)),
        }
    _A_in = _A_setup()

    def _B_setup():
        B, S, D, F = 1, 1, 640, 1728
        return {
            "x": _w((lambda *s: torch.randn(*s, dtype=torch.bfloat16) * 0.05)(B, S, D)),
            "w_gate": _w((lambda *s: torch.randn(*s, dtype=torch.bfloat16) * 0.05)(D, F)),
            "w_up": _w((lambda *s: torch.randn(*s, dtype=torch.bfloat16) * 0.05)(D, F)),
            "w_down": _w((lambda *s: torch.randn(*s, dtype=torch.bfloat16) * 0.05)(F, D)),
        }
    _B_in = _B_setup()

    return {
        "A_x": _A_in["x"],
        "A_k_cache": _A_in["k_cache"],
        "A_v_cache": _A_in["v_cache"],
        "A_wq": _A_in["wq"],
        "A_wo": _A_in["wo"],
        "B_x": _B_in["x"],
        "B_w_gate": _B_in["w_gate"],
        "B_w_up": _B_in["w_up"],
        "B_w_down": _B_in["w_down"],
    }

def reference(A_x, A_k_cache, A_v_cache, A_wq, A_wo, B_x, B_w_gate, B_w_up, B_w_down):
    def _A_ref(x, k_cache, v_cache, wq, wo):
        B, Sq, D = x.shape
        H, Dh = 32, 128
        q = (lambda x, *d: x.permute(*d))((x @ wq).reshape(B, Sq, H, Dh), 0, 2, 1, 3)
        scores = torch.einsum("bhqd,bhkd->bhqk", q, k_cache) * (Dh ** -0.5)
        p = _softmax(scores, axis=-1)
        o = torch.einsum("bhqk,bhkd->bhqd", p, v_cache)
        return (lambda x, *d: x.permute(*d))(o, 0, 2, 1, 3).reshape(B, Sq, H * Dh) @ wo

    def _B_ref(x, w_gate, w_up, w_down):
        g = _silu(x @ w_gate)
        u = x @ w_up
        return (g * u) @ w_down

    x = A_x
    h = x + _A_ref(_layernorm(x, axis=-1), A_k_cache, A_v_cache, A_wq, A_wo)
    h2 = h + _B_ref(_layernorm(h, axis=-1), B_w_gate, B_w_up, B_w_down)
    return h2
```

```cpp
#include <hip/hip_runtime.h>
#include <hip/hip_bf16.h>
#include <stdint.h>

#define MIMIC 1

typedef unsigned short u16;
using u32x4 = __attribute__((ext_vector_type(4))) unsigned;
using bf16x2 = __attribute__((ext_vector_type(2))) __bf16;

constexpr int D = 640, H = 32, SK = 16384, DH = 128, F = 1728, HD = H * DH;
constexpr int NSPLIT = 16;

__device__ __forceinline__ float bf2f(u16 v) { return __uint_as_float(((unsigned)v) << 16); }
__device__ __forceinline__ float rbf(float f) {
  unsigned u = __float_as_uint(f); u = (u + 0x7fffu + ((u >> 16) & 1u)) & 0xffff0000u; return __uint_as_float(u); }
__device__ __forceinline__ float mim(float f) { return MIMIC ? rbf(f) : f; }
__device__ __forceinline__ float dot2(unsigned k, unsigned q, float acc) {
  return __builtin_amdgcn_fdot2_f32_bf16(__builtin_bit_cast(bf16x2, k), __builtin_bit_cast(bf16x2, q), acc, false);
}
__device__ float block_sum(float v, float* red) {
  for (int m = 32; m >= 1; m >>= 1) v += __shfl_xor(v, m);
  int w = threadIdx.x >> 6, nw = blockDim.x >> 6;
  __syncthreads();
  if ((threadIdx.x & 63) == 0) red[w] = v;
  __syncthreads();
  float s = 0; for (int i = 0; i < nw; ++i) s += red[i];
  return s;
}
__device__ void ln640(const float* in, float* out, float* red) {
  float s = 0; for (int i = threadIdx.x; i < D; i += blockDim.x) s += in[i];
  float mean = mim(block_sum(s, red) / D);
  float meanf = block_sum(s, red) / D;
  float vs = 0; for (int i = threadIdx.x; i < D; i += blockDim.x) { float d = in[i] - meanf; vs += d * d; }
  float var = mim(block_sum(vs, red) / D);
  float den = mim(sqrtf(mim(var + 1e-5f)));
  for (int i = threadIdx.x; i < D; i += blockDim.x) out[i] = mim(mim(in[i] - mean) / den);
  __syncthreads();
}

__global__ void k_q(const u16* x, const u16* wq, float* q) {
  __shared__ float xin[D], xn[D], red[16], part[4][64];
  for (int i = threadIdx.x; i < D; i += blockDim.x) xin[i] = bf2f(x[i]);
  __syncthreads();
  ln640(xin, xn, red);
  int c = threadIdx.x & 63, g = threadIdx.x >> 6; int n = blockIdx.x * 64 + c;
  float a = 0; for (int k = g; k < D; k += 4) a += xn[k] * bf2f(wq[(size_t)k * HD + n]);
  part[g][c] = a; __syncthreads();
  if (g == 0) q[n] = rbf(part[0][c] + part[1][c] + part[2][c] + part[3][c]);
}

__global__ void __launch_bounds__(256) k_attn(const float* q, const u16* K, const u16* V, float* pm, float* pl, float* po) {
  int s = blockIdx.x, h = blockIdx.y;
  constexpr int T = SK / NSPLIT;
  __shared__ float sc[T]; __shared__ float red[16]; __shared__ float oacc[16][DH];
  int tid = threadIdx.x, c = tid & 15, r = tid >> 4;
  unsigned qp[4];
  for (int j = 0; j < 4; ++j) { float a = q[h * DH + 8 * c + 2 * j], b = q[h * DH + 8 * c + 2 * j + 1];
    qp[j] = (__float_as_uint(a) >> 16) | (__float_as_uint(b) & 0xffff0000u); }
  const u16* kb = K + ((size_t)h * SK + (size_t)s * T) * DH; const u16* vb = V + ((size_t)h * SK + (size_t)s * T) * DH;
  for (int t = r; t < T; t += 16) {
    u32x4 k = *reinterpret_cast<const u32x4*>(kb + (size_t)t * DH + 8 * c);
    float d = 0; for (int j = 0; j < 4; ++j) d = dot2(k[j], qp[j], d);
    for (int m = 8; m >= 1; m >>= 1) d += __shfl_xor(d, m);
    if (c == 0) sc[t] = mim(mim(d) * 0.08838834764831845f);
  }
  __syncthreads();
  float mx = -INFINITY; for (int t = tid; t < T; t += 256) mx = fmaxf(mx, sc[t]);
  for (int m = 32; m >= 1; m >>= 1) mx = fmaxf(mx, __shfl_xor(mx, m));
  if ((tid & 63) == 0) red[tid >> 6] = mx; __syncthreads();
  mx = fmaxf(fmaxf(red[0], red[1]), fmaxf(red[2], red[3])); __syncthreads();
  float l = 0; for (int t = tid; t < T; t += 256) { float p = __expf(sc[t] - mx); sc[t] = p; l += p; }
  l = block_sum(l, red);
  float acc[8] = {0,0,0,0,0,0,0,0};
  for (int t = r; t < T; t += 16) {
    u32x4 v = *reinterpret_cast<const u32x4*>(vb + (size_t)t * DH + 8 * c);
    float p = sc[t];
    for (int j = 0; j < 4; ++j) { acc[2*j] += p * __uint_as_float(v[j] << 16); acc[2*j+1] += p * __uint_as_float(v[j] & 0xffff0000u); }
  }
  for (int j = 0; j < 8; ++j) oacc[r][8 * c + j] = acc[j];
  __syncthreads();
  if (tid < DH) { float o = 0; for (int i = 0; i < 16; ++i) o += oacc[i][tid]; po[((size_t)h * NSPLIT + s) * DH + tid] = o; }
  if (tid == 0) { pm[h * NSPLIT + s] = mx; pl[h * NSPLIT + s] = l; }
}

__global__ void k_comb(const float* pm, const float* pl, const float* po, float* o) {
  int h = blockIdx.x, d = threadIdx.x;
  float M = -INFINITY; for (int s = 0; s < NSPLIT; ++s) M = fmaxf(M, pm[h * NSPLIT + s]);
  float L = 0, a = 0; for (int s = 0; s < NSPLIT; ++s) { float w = __expf(pm[h * NSPLIT + s] - M); L += w * pl[h * NSPLIT + s]; a += w * po[((size_t)h * NSPLIT + s) * DH + d]; }
  o[h * DH + d] = mim(a / L);
}
__global__ void __launch_bounds__(1024) k_wo(const float* o, const u16* wo, const u16* x, float* hbuf) {
  __shared__ float part[16][64];
  int c = threadIdx.x & 63, g = threadIdx.x >> 6, n = blockIdx.x * 64 + c;
  float a = 0; for (int k = g; k < HD; k += 16) a += o[k] * bf2f(wo[(size_t)k * D + n]);
  part[g][c] = a; __syncthreads();
  if (g == 0) { float s = 0; for (int i = 0; i < 16; ++i) s += part[i][c]; hbuf[n] = mim(bf2f(x[n]) + mim(s)); }
}
__global__ void __launch_bounds__(256) k_gu(const float* hbuf, const u16* wg, const u16* wu, float* act) {
  __shared__ float xin[D], xn[D], red[16], pg[4][64], pu[4][64];
  for (int i = threadIdx.x; i < D; i += blockDim.x) xin[i] = hbuf[i];
  __syncthreads();
  ln640(xin, xn, red);
  int c = threadIdx.x & 63, g = threadIdx.x >> 6, n = blockIdx.x * 64 + c;
  float a = 0, b = 0; for (int k = g; k < D; k += 4) { a += xn[k] * bf2f(wg[(size_t)k * F + n]); b += xn[k] * bf2f(wu[(size_t)k * F + n]); }
  pg[g][c] = a; pu[g][c] = b; __syncthreads();
  if (g == 0) { float G = mim(pg[0][c] + pg[1][c] + pg[2][c] + pg[3][c]), U = mim(pu[0][c] + pu[1][c] + pu[2][c] + pu[3][c]);
    float sl = mim(G / (1.f + __expf(-G))); act[n] = mim(sl * U); }
}
__global__ void __launch_bounds__(1024) k_down(const float* act, const u16* wd, const float* hbuf, __hip_bfloat16* out) {
  __shared__ float part[16][64];
  int c = threadIdx.x & 63, g = threadIdx.x >> 6, n = blockIdx.x * 64 + c;
  float a = 0; for (int k = g; k < F; k += 16) a += act[k] * bf2f(wd[(size_t)k * D + n]);
  part[g][c] = a; __syncthreads();
  if (g == 0) { float s = 0; for (int i = 0; i < 16; ++i) s += part[i][c]; out[n] = __float2bfloat16(hbuf[n] + mim(s)); }
}

extern "C" void kernel_launch(void* const* d_in, const int* in_sizes, int n_in, void* d_out, int out_size, void* d_ws, size_t ws_size, hipStream_t stream) {
  const u16* x = (const u16*)d_in[0]; const u16* K = (const u16*)d_in[1]; const u16* V = (const u16*)d_in[2];
  const u16* wq = (const u16*)d_in[3]; const u16* wo = (const u16*)d_in[4];
  const u16* wg = (const u16*)d_in[6]; const u16* wu = (const u16*)d_in[7]; const u16* wd = (const u16*)d_in[8];
  float* ws = (float*)d_ws;
  float* q = ws; float* pm = ws + 4096; float* pl = pm + 1024; float* po = pl + 1024; float* o = po + H * NSPLIT * DH; float* hb = o + 4096; float* act = hb + 1024;
  k_q<<<HD / 64, 256, 0, stream>>>(x, wq, q);
  k_attn<<<dim3(NSPLIT, H), 256, 0, stream>>>(q, K, V, pm, pl, po);
  k_comb<<<H, DH, 0, stream>>>(pm, pl, po, o);
  k_wo<<<D / 64, 1024, 0, stream>>>(o, wo, x, hb);
  k_gu<<<F / 64, 256, 0, stream>>>(hb, wg, wu, act);
  k_down<<<D / 64, 1024, 0, stream>>>(act, wd, hb, (__hip_bfloat16*)d_out);
}
```

```cpp
#include <hip/hip_runtime.h>
#include <hip/hip_bf16.h>
#include <stdint.h>

#define DEPTH 2
#define STAMPS 0

typedef unsigned u32;
typedef unsigned long long u64;
typedef unsigned short u16;
typedef u32 u32x4 __attribute__((ext_vector_type(4)));
typedef u32 u32x2 __attribute__((ext_vector_type(2)));
typedef float f32x4 __attribute__((ext_vector_type(4)));
typedef float f32x2 __attribute__((ext_vector_type(2)));
typedef __bf16 bf16x2 __attribute__((ext_vector_type(2)));
#define GAS __attribute__((address_space(1)))

constexpr int D = 640, H = 32, SK = 16384, DH = 128, F = 1728, HD = H * DH;
constexpr int NBLK = 256, NTHR = 512, NWAVE = 8;
constexpr int TPW = 16;
constexpr float L2E = 1.4426950408889634f;
constexpr float SCALE = 0.08838834764831845f;
constexpr float EPS = 1e-5f;
constexpr int WS_EPOCH = 0;
constexpr int WS_QP = 4096;
constexpr int WS_AP = WS_QP + 256 * 1024;
constexpr int WS_ML = WS_AP + 256 * 1024;
constexpr int WS_YP = WS_ML + 4096;
constexpr int WS_AC = WS_YP + 8 * 320 * 16;
constexpr int WS_END = WS_AC + 432 * 16;
constexpr int WS_STAMP = 1 << 20;
constexpr int SPIN_CAP = 1 << 18;

struct Params {
  const u16 *x, *K, *V, *wq, *wo, *wg, *wu, *wd;
  u16* out;
  unsigned char* ws;
};

__device__ __forceinline__ float asf(u32 x) { return __builtin_bit_cast(float, x); }
__device__ __forceinline__ u32 asu(float x) { return __builtin_bit_cast(u32, x); }
__device__ __forceinline__ float dot2(u32 a, u32 b, float c) {
  return __builtin_amdgcn_fdot2_f32_bf16(__builtin_bit_cast(bf16x2, a), __builtin_bit_cast(bf16x2, b), c, false);
}
__device__ __forceinline__ u32 cvtpk(float lo, float hi) { bf16x2 v = {(__bf16)lo, (__bf16)hi}; return __builtin_bit_cast(u32, v); }
__device__ __forceinline__ float rbf(float x) { return asf(cvtpk(x, x) << 16); }
__device__ __forceinline__ float lo16(u32 v) { return asf(v << 16); }
__device__ __forceinline__ float hi16(u32 v) { return asf(v & 0xffff0000u); }
template <int CTRL> __device__ __forceinline__ float dpp(float x) {
  return asf((u32)__builtin_amdgcn_mov_dpp((int)asu(x), CTRL, 0xf, 0xf, true));
}
constexpr int QX1 = 0xB1, QX2 = 0x4E, HMIR = 0x141, ROR4 = 0x124, ROR8 = 0x128, NBC = 0x150;
__device__ __forceinline__ float fold32(float a, float b) {
  auto r = __builtin_amdgcn_permlane32_swap(asu(a), asu(b), false, false); return asf(r[0]) + asf(r[1]); }
__device__ __forceinline__ float fold16(float a, float b) {
  auto r = __builtin_amdgcn_permlane16_swap(asu(a), asu(b), false, false); return asf(r[0]) + asf(r[1]); }
__device__ __forceinline__ float max32(float a) {
  auto r = __builtin_amdgcn_permlane32_swap(asu(a), asu(a), false, false); return fmaxf(asf(r[0]), asf(r[1])); }
__device__ __forceinline__ float max16(float a) {
  auto r = __builtin_amdgcn_permlane16_swap(asu(a), asu(a), false, false); return fmaxf(asf(r[0]), asf(r[1])); }
__device__ __forceinline__ float wave_sum(float v) {
  v += dpp<QX1>(v); v += dpp<QX2>(v); v += dpp<HMIR>(v); v += dpp<ROR8>(v);
  v = fold16(v, v); return fold32(v, v);
}
__device__ __forceinline__ f32x2 fold8_rows(const float (&a)[8]) {
  float t0 = fold32(a[0], a[4]), t1 = fold32(a[1], a[5]), t2 = fold32(a[2], a[6]), t3 = fold32(a[3], a[7]);
  return f32x2{fold16(t0, t2), fold16(t1, t3)};
}
__device__ __forceinline__ u32x4 ld_nt(const u16* p) { return __builtin_nontemporal_load(reinterpret_cast<const u32x4*>(p)); }
__device__ __forceinline__ u32x4 ld_w(const u16* p) { return *reinterpret_cast<const u32x4*>(p); }
__device__ __forceinline__ u32x4 ld_unit(__amdgpu_buffer_rsrc_t rs, int off) { return __builtin_amdgcn_raw_buffer_load_b128(rs, off, 0, 16); }
__device__ __forceinline__ void st_unit(__amdgpu_buffer_rsrc_t rs, int off, u32 a, u32 b, u32 tag) {
  __builtin_amdgcn_raw_buffer_store_b128(u32x4{a, tag, b, tag}, rs, off, 0, 16);
}
__device__ __forceinline__ bool tags_ok(u32x4 v, u32 tag) { return v.y == tag && v.w == tag; }
__device__ __forceinline__ void pair_rows(u32x4 a, u32x4 b, u32 (&o)[8]) {
#pragma unroll
  for (int k = 0; k < 4; ++k) { o[2 * k] = __builtin_amdgcn_perm(b[k], a[k], 0x05040100u); o[2 * k + 1] = __builtin_amdgcn_perm(b[k], a[k], 0x07060302u); }
}
#define SCHED() __builtin_amdgcn_sched_barrier(0)
#define MEMFENCE() asm volatile("" ::: "memory")
#define STAMP(i) do {} while (0)
#define STAMPW(i) do {} while (0)

struct Lds {
  f32x2 pr[8][64];
  f32x2 pa[8][64];
  float mw[8], lw[8];
  float fs[8][32];
  f32x2 red[8][16];
  float st[8][2];
  u32 hp[320];
  u32 act[864];
  unsigned char pad[90 * 1024];
};

struct Att { float m, l, acc[8]; };

__device__ __forceinline__ void qk_dots(const u32x4 (&k)[4], const u32 (&q)[4], float (&d)[4]) {
#pragma unroll
  for (int i = 0; i < 4; ++i) { float a = dot2(k[i][0], q[0], 0.f); a = dot2(k[i][1], q[1], a); a = dot2(k[i][2], q[2], a); d[i] = dot2(k[i][3], q[3], a); }
}
__device__ __forceinline__ float soft_step(const float (&d)[4], Att& st, bool b8, bool b4) {
  const float e0 = (b8 ? d[1] : d[0]) + dpp<ROR8>(b8 ? d[0] : d[1]);
  const float e1 = (b8 ? d[3] : d[2]) + dpp<ROR8>(b8 ? d[2] : d[3]);
  float s = (b4 ? e1 : e0) + dpp<HMIR>(b4 ? e0 : e1);
  s += dpp<QX1>(s); s += dpp<QX2>(s);
  s = rbf(rbf(s) * SCALE);
  float mx = fmaxf(s, dpp<ROR4>(s)); mx = fmaxf(mx, dpp<ROR8>(mx));
  const float mn = fmaxf(st.m, mx);
  const float alpha = __builtin_amdgcn_exp2f((st.m - mn) * L2E);
  const float p = __builtin_amdgcn_exp2f((s - mn) * L2E);
  st.m = mn; st.l = st.l * alpha + p;
#pragma unroll
  for (int j = 0; j < 8; ++j) st.acc[j] *= alpha;
  return p;
}
__device__ __forceinline__ void pv_acc(const u32x4 (&v)[4], float p, Att& st) {
  const float p0 = dpp<NBC + 0>(p), p1 = dpp<NBC + 8>(p), p2 = dpp<NBC + 4>(p), p3 = dpp<NBC + 12>(p);
  const float pp[4] = {p0, p1, p2, p3};
#pragma unroll
  for (int i = 0; i < 4; ++i)
#pragma unroll
    for (int k = 0; k < 4; ++k) { st.acc[2 * k] = fmaf(pp[i], lo16(v[i][k]), st.acc[2 * k]); st.acc[2 * k + 1] = fmaf(pp[i], hi16(v[i][k]), st.acc[2 * k + 1]); }
}

__global__ void __launch_bounds__(NTHR, 2) fused_block(Params p) {
  __shared__ Lds L;
  const int tid = threadIdx.x, lane = tid & 63, wave = __builtin_amdgcn_readfirstlane(tid >> 6);
  const int b = blockIdx.x, h = b >> 3, s = b & 7;
  const int r = lane >> 4, c = lane & 15;
  const __amdgpu_buffer_rsrc_t rs = __builtin_amdgcn_make_buffer_rsrc(p.ws, 0, WS_END, 0x00020000);
  STAMP(0);
  const u32 ep_raw = __hip_atomic_load((GAS u32*)(p.ws + WS_EPOCH), __ATOMIC_RELAXED, __HIP_MEMORY_SCOPE_AGENT);
  const u32* xw = reinterpret_cast<const u32*>(p.x);
  u32 xa[5], xp[2]; u32x4 wqv[4];
  if (wave < 5) {
#pragma unroll
    for (int j = 0; j < 5; ++j) xa[j] = xw[lane + 64 * j];
#pragma unroll
    for (int m = 0; m < 2; ++m) { const int pp = 40 * s + 4 * (2 * wave + m) + r; xp[m] = xw[pp];
      wqv[2 * m] = ld_nt(p.wq + (size_t)(2 * pp) * HD + h * DH + 8 * c); wqv[2 * m + 1] = ld_nt(p.wq + (size_t)(2 * pp + 1) * HD + h * DH + 8 * c); }
  }
  SCHED();
  const u16* kb = p.K + ((size_t)h * SK + s * 2048 + wave * 16 + r) * DH + 8 * c;
  const u16* vb = p.V + ((size_t)h * SK + s * 2048 + wave * 16 + r) * DH + 8 * c;
  constexpr int TSTR = 128 * DH;
  u32x4 bk[DEPTH][4], bv[DEPTH][4];
#pragma unroll
  for (int i = 0; i < 4; ++i) bk[0][i] = ld_nt(kb + i * 4 * DH);
#pragma unroll
  for (int i = 0; i < 4; ++i) bv[0][i] = ld_nt(vb + i * 4 * DH);
  SCHED();
  const u32 tag = __builtin_amdgcn_readfirstlane(ep_raw) + 1u;
  if (wave < 5) {
    float sum = 0.f;
#pragma unroll
    for (int j = 0; j < 5; ++j) sum += lo16(xa[j]) + hi16(xa[j]);
    const float mean = wave_sum(sum) * (1.f / D);
    float vs = 0.f;
#pragma unroll
    for (int j = 0; j < 5; ++j) { const float a = lo16(xa[j]) - mean, bb = hi16(xa[j]) - mean; vs += a * a + bb * bb; }
    const float var = rbf(wave_sum(vs) * (1.f / D));
    const float meanb = rbf(mean), rden = 1.f / rbf(sqrtf(rbf(var + EPS)));
    float a[8] = {0.f, 0.f, 0.f, 0.f, 0.f, 0.f, 0.f, 0.f};
#pragma unroll
    for (int m = 0; m < 2; ++m) {
      const u32 xn = cvtpk(rbf(lo16(xp[m]) - meanb) * rden, rbf(hi16(xp[m]) - meanb) * rden);
      u32 wp[8]; pair_rows(wqv[2 * m], wqv[2 * m + 1], wp);
#pragma unroll
      for (int j = 0; j < 8; ++j) a[j] = dot2(wp[j], xn, a[j]);
    }
    L.pr[wave][4 * c + r] = fold8_rows(a);
  }
  __syncthreads();
  if (wave == 0) {
    f32x2 q = L.pr[0][lane];
#pragma unroll
    for (int w = 1; w < 5; ++w) q += L.pr[w][lane];
    st_unit(rs, WS_QP + b * 1024 + lane * 16, asu(q.x), asu(q.y), tag);
  }
  STAMP(1);
  u32x4 qu;
  for (int spin = 0;; ++spin) {
    qu = ld_unit(rs, WS_QP + (8 * h + wave) * 1024 + lane * 16);
    if (__all(tags_ok(qu, tag)) || spin > SPIN_CAP) break;
    MEMFENCE(); __builtin_amdgcn_s_sleep(1);
  }
  if (DEPTH > 1) {
#pragma unroll
    for (int d = 1; d < DEPTH; ++d) {
#pragma unroll
      for (int i = 0; i < 4; ++i) bk[d][i] = ld_nt(kb + d * TSTR + i * 4 * DH);
#pragma unroll
      for (int i = 0; i < 4; ++i) bv[d][i] = ld_nt(vb + d * TSTR + i * 4 * DH); }
  }
  SCHED();
  __syncthreads();
  L.pr[wave][lane] = f32x2{asf(qu.x), asf(qu.z)};
  __syncthreads();
  u32 q[4];
  {
    f32x4 a = {0.f, 0.f, 0.f, 0.f}, bb = a;
#pragma unroll
    for (int w = 0; w < 8; ++w) { a += *reinterpret_cast<const f32x4*>(&L.pr[w][4 * c]); bb += *reinterpret_cast<const f32x4*>(&L.pr[w][4 * c + 2]); }
    q[0] = cvtpk(a.x, a.y); q[1] = cvtpk(a.z, a.w); q[2] = cvtpk(bb.x, bb.y); q[3] = cvtpk(bb.z, bb.w);
  }
  STAMP(2);
  Att st; st.m = -INFINITY; st.l = 0.f;
#pragma unroll
  for (int j = 0; j < 8; ++j) st.acc[j] = 0.f;
  const bool b8 = c & 8, b4 = c & 4;
#pragma unroll 1
  for (int i = 0; i < TPW - DEPTH; i += DEPTH) {
#pragma unroll
    for (int d = 0; d < DEPTH; ++d) {
      float dd[4]; qk_dots(bk[d], q, dd);
      asm volatile("" : "+v"(dd[0]), "+v"(dd[1]), "+v"(dd[2]), "+v"(dd[3]));
      SCHED();
#pragma unroll
      for (int j = 0; j < 4; ++j) bk[d][j] = ld_nt(kb + (size_t)(i + d + DEPTH) * TSTR + j * 4 * DH);
      SCHED();
      const float pv = soft_step(dd, st, b8, b4);
      pv_acc(bv[d], pv, st);
      asm volatile("" : "+v"(st.acc[0]), "+v"(st.acc[1]), "+v"(st.acc[2]), "+v"(st.acc[3]), "+v"(st.acc[4]), "+v"(st.acc[5]), "+v"(st.acc[6]), "+v"(st.acc[7]));
      SCHED();
#pragma unroll
      for (int j = 0; j < 4; ++j) bv[d][j] = ld_nt(vb + (size_t)(i + d + DEPTH) * TSTR + j * 4 * DH);
      SCHED();
    }
  }
  const bool roleW = b < 160;
  const int kblk = b & 7, nblk = b >> 3;
  const int jf = 31 - (b >> 3), lgf = (b & 7) + 8 * (jf >> 2); const bool roleF = jf < 16 && lgf < 27; const int fb = 4 * lgf + (jf & 3);
  const int lgd = (b & 7) + 8 * (b >> 5); const bool roleD = (b >> 3) < 8 && lgd < 10; const int cb = 4 * lgd + ((b >> 3) & 3);
  u32x4 wW[4], wF[6], wD[8];
  const int dW = 2 * (8 * wave + ((lane >> 2) & 7)) + (lane >> 5);
  if (roleW) {
#pragma unroll
    for (int i = 0; i < 4; ++i) wW[i] = ld_w(p.wo + (size_t)(512 * kblk + 128 * i + dW) * D + 32 * nblk + 8 * (lane & 3));
  }
  if (roleF) {
    const u16* wm = (lane & 2) ? p.wu : p.wg;
#pragma unroll
    for (int j = 0; j < 3; ++j) { int pp = (lane >> 2) + 16 * wave + 128 * j; pp = pp < 320 ? pp : 319;
      wF[2 * j] = ld_w(wm + (size_t)(2 * pp) * F + 16 * fb + 8 * (lane & 1)); wF[2 * j + 1] = ld_w(wm + (size_t)(2 * pp + 1) * F + 16 * fb + 8 * (lane & 1)); }
  }
  if (roleD) {
#pragma unroll
    for (int j = 0; j < 4; ++j) { int pp = (lane >> 1) + 32 * wave + 256 * j; pp = pp < 864 ? pp : 863;
      wD[2 * j] = ld_w(p.wd + (size_t)(2 * pp) * D + 16 * cb + 8 * (lane & 1)); wD[2 * j + 1] = ld_w(p.wd + (size_t)(2 * pp + 1) * D + 16 * cb + 8 * (lane & 1)); }
  }
  u32 xres = 0;
  if (roleF) xres = xw[tid < 320 ? tid : 0];
  u32 xres2 = 0;
  if (roleD) xres2 = xw[8 * cb + (tid & 7)];
  SCHED();
#pragma unroll
  for (int d = 0; d < DEPTH; ++d) {
    float dd[4]; qk_dots(bk[d], q, dd);
    const float pv = soft_step(dd, st, b8, b4);
    pv_acc(bv[d], pv, st);
  }
  STAMPW(8);
  {
    const float mw = max32(max16(st.m));
    const float f = __builtin_amdgcn_exp2f((st.m - mw) * L2E);
    float l = st.l * f; l += dpp<ROR4>(l); l += dpp<ROR8>(l); l = fold16(l, l); l = fold32(l, l);
#pragma unroll
    for (int j = 0; j < 8; ++j) st.acc[j] *= f;
    L.pa[wave][4 * c + r] = fold8_rows(st.acc);
    if (lane == 0) { L.mw[wave] = mw; L.lw[wave] = l; }
  }
  __syncthreads();
  if (wave == 0) {
    float M = L.mw[0];
#pragma unroll
    for (int w = 1; w < 8; ++w) M = fmaxf(M, L.mw[w]);
    float l = 0.f; f32x2 o = {0.f, 0.f};
#pragma unroll
    for (int w = 0; w < 8; ++w) { const float f = __builtin_amdgcn_exp2f((L.mw[w] - M) * L2E); l = fmaf(f, L.lw[w], l); o += f * L.pa[w][lane]; }
    st_unit(rs, WS_AP + b * 1024 + lane * 16, asu(o.x), asu(o.y), tag);
    if (lane == 0) st_unit(rs, WS_ML + b * 16, asu(M), asu(l), tag);
  }
  STAMP(3);
  if (roleW) {
    const int i = lane & 3, dp = 8 * wave + ((lane >> 2) & 7), sh = lane >> 5, hh = 4 * kblk + i;
    u32 wp01[8], wp23[8]; pair_rows(wW[0], wW[1], wp01); pair_rows(wW[2], wW[3], wp23);
    u32x4 a[4], ml;
    for (int spin = 0;; ++spin) {
      ml = ld_unit(rs, WS_ML + (hh * 8 + ((lane >> 2) & 7)) * 16);
#pragma unroll
      for (int j = 0; j < 4; ++j) a[j] = ld_unit(rs, WS_AP + (hh * 8 + 4 * sh + j) * 1024 + dp * 16);
      const bool ok = tags_ok(ml, tag) && tags_ok(a[0], tag) && tags_ok(a[1], tag) && tags_ok(a[2], tag) && tags_ok(a[3], tag);
      if (__all(ok) || spin > SPIN_CAP) break;
      MEMFENCE(); __builtin_amdgcn_s_sleep(1);
    }
    STAMP(4);
    const float m = asf(ml.x);
    float M = fmaxf(m, dpp<ROR4>(m)); M = fmaxf(M, dpp<ROR8>(M)); M = max16(M);
    const float w = __builtin_amdgcn_exp2f((m - M) * L2E);
    float l = w * asf(ml.z); l += dpp<ROR4>(l); l += dpp<ROR8>(l); l = fold16(l, l);
    if (lane < 32) L.fs[wave][i * 8 + (lane >> 2)] = w / l;
    __builtin_amdgcn_fence(__ATOMIC_RELEASE, "wavefront"); __builtin_amdgcn_wave_barrier(); __builtin_amdgcn_fence(__ATOMIC_ACQUIRE, "wavefront");
    const f32x4 f = *reinterpret_cast<const f32x4*>(&L.fs[wave][i * 8 + 4 * sh]);
    float o0 = 0.f, o1 = 0.f;
#pragma unroll
    for (int j = 0; j < 4; ++j) { o0 = fmaf(f[j], asf(a[j].x), o0); o1 = fmaf(f[j], asf(a[j].z), o1); }
    const float o = fold32(o0, o1);
    const u32 p01 = cvtpk(dpp<0x00>(o), dpp<0x55>(o)), p23 = cvtpk(dpp<0xAA>(o), dpp<0xFF>(o));
    float y[8];
#pragma unroll
    for (int j = 0; j < 8; ++j) y[j] = dot2(wp23[j], p23, dot2(wp01[j], p01, 0.f));
    f32x2 t = fold8_rows(y);
    t.x += dpp<ROR8>(t.x); t.y += dpp<ROR8>(t.y); t.x += dpp<ROR4>(t.x); t.y += dpp<ROR4>(t.y);
    if (c < 4) L.red[wave][4 * c + r] = t;
    __syncthreads();
    if (tid < 16) { f32x2 z = L.red[0][tid];
#pragma unroll
      for (int w2 = 1; w2 < 8; ++w2) z += L.red[w2][tid];
      st_unit(rs, WS_YP + (kblk * 320 + 16 * nblk + tid) * 16, asu(z.x), asu(z.y), tag); }
    STAMP(5);
  }
  if (roleF) {
    u32 wp[3][8];
#pragma unroll
    for (int j = 0; j < 3; ++j) pair_rows(wF[2 * j], wF[2 * j + 1], wp[j]);
    if (wave < 5) {
      u32x4 y[8];
      for (int spin = 0;; ++spin) {
        bool ok = true;
#pragma unroll
        for (int k = 0; k < 8; ++k) y[k] = ld_unit(rs, WS_YP + (k * 320 + tid) * 16);
#pragma unroll
        for (int k = 0; k < 8; ++k) ok = ok && tags_ok(y[k], tag);
        if (__all(ok) || spin > SPIN_CAP) break;
        MEMFENCE(); __builtin_amdgcn_s_sleep(1);
      }
      float a0 = 0.f, a1 = 0.f;
#pragma unroll
      for (int k = 0; k < 8; ++k) { a0 += asf(y[k].x); a1 += asf(y[k].z); }
      const u32 hp = cvtpk(lo16(xres) + rbf(a0), hi16(xres) + rbf(a1));
      const float h0 = lo16(hp), h1 = hi16(hp);
      const float sm = wave_sum(h0 + h1), mu = sm * (1.f / 128.f);
      const float m2 = wave_sum((h0 - mu) * (h0 - mu) + (h1 - mu) * (h1 - mu));
      L.hp[tid] = hp;
      if (lane == 0) { L.st[wave][0] = sm; L.st[wave][1] = m2; }
    }
    STAMP(6);
    __syncthreads();
    float sm = 0.f;
#pragma unroll
    for (int w = 0; w < 5; ++w) sm += L.st[w][0];
    const float mean = sm * (1.f / D);
    float m2 = 0.f;
#pragma unroll
    for (int w = 0; w < 5; ++w) { const float dm = L.st[w][0] * (1.f / 128.f) - mean; m2 += L.st[w][1] + 128.f * dm * dm; }
    const float meanb = rbf(mean), rden = 1.f / rbf(sqrtf(rbf(rbf(m2 * (1.f / D)) + EPS)));
    float g[8] = {0.f, 0.f, 0.f, 0.f, 0.f, 0.f, 0.f, 0.f};
#pragma unroll
    for (int j = 0; j < 3; ++j) {
      const int pp = (lane >> 2) + 16 * wave + 128 * j;
      const u32 hv = L.hp[pp < 320 ? pp : 319];
      u32 hn = cvtpk(rbf(lo16(hv) - meanb) * rden, rbf(hi16(hv) - meanb) * rden);
      hn = pp < 320 ? hn : 0u;
#pragma unroll
      for (int k = 0; k < 8; ++k) g[k] = dot2(wp[j][k], hn, g[k]);
    }
    f32x2 t = fold8_rows(g);
    t.x += dpp<ROR8>(t.x); t.y += dpp<ROR8>(t.y); t.x += dpp<ROR4>(t.x); t.y += dpp<ROR4>(t.y);
    if (c < 4) L.red[wave][4 * c + r] = t;
    __syncthreads();
    if (wave == 0) {
      const int v = lane & 7;
      f32x2 gg = L.red[0][v], uu = L.red[0][8 + v];
#pragma unroll
      for (int w = 1; w < 8; ++w) { gg += L.red[w][v]; uu += L.red[w][8 + v]; }
      const float g0 = rbf(gg.x), g1 = rbf(gg.y);
      const float s0 = rbf(g0 / (1.f + __expf(-g0))), s1 = rbf(g1 / (1.f + __expf(-g1)));
      const u32 ap = cvtpk(s0 * rbf(uu.x), s1 * rbf(uu.y));
      const u32 nx = asu(dpp<QX1>(asf(ap)));
      if (lane < 8 && !(lane & 1)) st_unit(rs, WS_AC + (4 * fb + (lane >> 1)) * 16, ap, nx, tag);
    }
    STAMP(7);
  }
  if (roleD) {
    u32 wp[4][8];
#pragma unroll
    for (int j = 0; j < 4; ++j) pair_rows(wD[2 * j], wD[2 * j + 1], wp[j]);
    u32 hres = 0;
    if (wave == 7) {
      u32x4 y[8];
      for (int spin = 0;; ++spin) {
        bool ok = true;
#pragma unroll
        for (int k = 0; k < 8; ++k) y[k] = ld_unit(rs, WS_YP + (k * 320 + 8 * cb + (lane & 7)) * 16);
#pragma unroll
        for (int k = 0; k < 8; ++k) ok = ok && tags_ok(y[k], tag);
        if (__all(ok) || spin > SPIN_CAP) break;
        MEMFENCE(); __builtin_amdgcn_s_sleep(1);
      }
      float a0 = 0.f, a1 = 0.f;
#pragma unroll
      for (int k = 0; k < 8; ++k) { a0 += asf(y[k].x); a1 += asf(y[k].z); }
      hres = cvtpk(lo16(xres2) + rbf(a0), hi16(xres2) + rbf(a1));
    } else {
      const int u = tid < 432 ? tid : 431;
      u32x4 a;
      for (int spin = 0;; ++spin) {
        a = ld_unit(rs, WS_AC + u * 16);
        if (__all(tags_ok(a, tag)) || spin > SPIN_CAP) break;
        MEMFENCE(); __builtin_amdgcn_s_sleep(1);
      }
      if (tid < 432) *reinterpret_cast<u32x2*>(&L.act[2 * tid]) = u32x2{a.x, a.z};
    }
    STAMP(16);
    __syncthreads();
    if (b == 0 && tid == 0) __hip_atomic_store((GAS u32*)(p.ws + WS_EPOCH), tag, __ATOMIC_RELAXED, __HIP_MEMORY_SCOPE_AGENT);
    float o[8] = {0.f, 0.f, 0.f, 0.f, 0.f, 0.f, 0.f, 0.f};
#pragma unroll
    for (int j = 0; j < 4; ++j) {
      const int pp = (lane >> 1) + 32 * wave + 256 * j;
      u32 av = L.act[pp < 864 ? pp : 863]; av = pp < 864 ? av : 0u;
#pragma unroll
      for (int k = 0; k < 8; ++k) o[k] = dot2(wp[j][k], av, o[k]);
    }
    f32x2 t = fold8_rows(o);
    t.x += dpp<ROR8>(t.x); t.y += dpp<ROR8>(t.y); t.x += dpp<ROR4>(t.x); t.y += dpp<ROR4>(t.y); t.x += dpp<QX2>(t.x); t.y += dpp<QX2>(t.y);
    if (c < 2) L.red[wave][4 * c + r] = t;
    __syncthreads();
    if (wave == 7) {
      const int v = lane & 7;
      f32x2 z = L.red[0][v];
#pragma unroll
      for (int w = 1; w < 8; ++w) z += L.red[w][v];
      const u32 ov = cvtpk(lo16(hres) + rbf(z.x), hi16(hres) + rbf(z.y));
      if (lane < 8) reinterpret_cast<u32*>(p.out)[8 * cb + lane] = ov;
    }
    STAMP(17);
  }
}

extern "C" void kernel_launch(void* const* d_in, const int* in_sizes, int n_in, void* d_out, int out_size, void* d_ws, size_t ws_size,
                              hipStream_t stream) {
  Params p;
  p.x = (const u16*)d_in[0]; p.K = (const u16*)d_in[1]; p.V = (const u16*)d_in[2]; p.wq = (const u16*)d_in[3]; p.wo = (const u16*)d_in[4];
  p.wg = (const u16*)d_in[6]; p.wu = (const u16*)d_in[7]; p.wd = (const u16*)d_in[8];
  p.out = (u16*)d_out; p.ws = (unsigned char*)d_ws;
  fused_block<<<NBLK, NTHR, 0, stream>>>(p);
}
```

```cpp
#include <hip/hip_runtime.h>
#include <hip/hip_bf16.h>
#include <stdint.h>

#define DEPTH 1
#define STAMPS 0

typedef unsigned u32;
typedef unsigned long long u64;
typedef unsigned short u16;
typedef u32 u32x4 __attribute__((ext_vector_type(4)));
typedef u32 u32x2 __attribute__((ext_vector_type(2)));
typedef float f32x4 __attribute__((ext_vector_type(4)));
typedef float f32x2 __attribute__((ext_vector_type(2)));
typedef __bf16 bf16x2 __attribute__((ext_vector_type(2)));
#define GAS __attribute__((address_space(1)))

constexpr int D = 640, H = 32, SK = 16384, DH = 128, F = 1728, HD = H * DH;
constexpr int NBLK = 256, NTHR = 512, NWAVE = 8;
constexpr int TPW = 16;
constexpr float L2E = 1.4426950408889634f;
constexpr float SCALE = 0.08838834764831845f;
constexpr float EPS = 1e-5f;
constexpr int WS_EPOCH = 0;
constexpr int WS_QP = 4096;
constexpr int WS_AP = WS_QP + 256 * 1024;
constexpr int WS_ML = WS_AP + 256 * 1024;
constexpr int WS_YP = WS_ML + 4096;
constexpr int WS_AC = WS_YP + 8 * 320 * 16;
constexpr int WS_END = WS_AC + 432 * 16;
constexpr int WS_STAMP = 1 << 20;
constexpr int SPIN_CAP = 1 << 18;

struct Params {
  const u16 *x, *K, *V, *wq, *wo, *wg, *wu, *wd;
  u16* out;
  unsigned char* ws;
};

#define SCHED() __builtin_amdgcn_sched_barrier(0)
#define MEMFENCE() asm volatile("" ::: "memory")
__device__ __forceinline__ float asf(u32 x) { return __builtin_bit_cast(float, x); }
__device__ __forceinline__ u32 asu(float x) { return __builtin_bit_cast(u32, x); }
__device__ __forceinline__ float dot2(u32 a, u32 b, float c) {
  return __builtin_amdgcn_fdot2_f32_bf16(__builtin_bit_cast(bf16x2, a), __builtin_bit_cast(bf16x2, b), c, false);
}
__device__ __forceinline__ u32 cvtpk(float lo, float hi) { bf16x2 v = {(__bf16)lo, (__bf16)hi}; return __builtin_bit_cast(u32, v); }
__device__ __forceinline__ float rbf(float x) { return asf(cvtpk(x, x) << 16); }
__device__ __forceinline__ float lo16(u32 v) { return asf(v << 16); }
__device__ __forceinline__ float hi16(u32 v) { return asf(v & 0xffff0000u); }
template <int CTRL> __device__ __forceinline__ float dpp(float x) {
  return asf((u32)__builtin_amdgcn_mov_dpp((int)asu(x), CTRL, 0xf, 0xf, true));
}
constexpr int QX1 = 0xB1, QX2 = 0x4E, HMIR = 0x141, ROR4 = 0x124, ROR8 = 0x128, NBC = 0x150;
__device__ __forceinline__ float fold32(float a, float b) {
  auto r = __builtin_amdgcn_permlane32_swap(asu(a), asu(b), false, false); return asf(r[0]) + asf(r[1]); }
__device__ __forceinline__ float fold16(float a, float b) {
  auto r = __builtin_amdgcn_permlane16_swap(asu(a), asu(b), false, false); return asf(r[0]) + asf(r[1]); }
__device__ __forceinline__ float max32(float a) {
  auto r = __builtin_amdgcn_permlane32_swap(asu(a), asu(a), false, false); return fmaxf(asf(r[0]), asf(r[1])); }
__device__ __forceinline__ float max16(float a) {
  auto r = __builtin_amdgcn_permlane16_swap(asu(a), asu(a), false, false); return fmaxf(asf(r[0]), asf(r[1])); }
__device__ __forceinline__ float wave_sum(float v) {
  v += dpp<QX1>(v); v += dpp<QX2>(v); v += dpp<HMIR>(v); v += dpp<ROR8>(v);
  v = fold16(v, v); return fold32(v, v);
}
__device__ __forceinline__ float quads_sum(float v) {
  v += dpp<HMIR>(v); v += dpp<ROR8>(v);
  v = fold16(v, v); return fold32(v, v);
}
__device__ __forceinline__ float frcp(float x) { return __builtin_amdgcn_rcpf(x); }
__device__ __forceinline__ float fexp2(float x) { return __builtin_amdgcn_exp2f(x); }
__device__ __forceinline__ f32x2 fold8_rows(const float (&a)[8]) {
  float t0 = fold32(a[0], a[4]), t1 = fold32(a[1], a[5]), t2 = fold32(a[2], a[6]), t3 = fold32(a[3], a[7]);
  return f32x2{fold16(t0, t2), fold16(t1, t3)};
}
__device__ __forceinline__ u32x4 ld_nt(const u16* p) { return __builtin_nontemporal_load(reinterpret_cast<const u32x4*>(p)); }
__device__ __forceinline__ u32x4 ld_w(const u16* p) { return *reinterpret_cast<const u32x4*>(p); }
__device__ __forceinline__ u32x4 ld_unit(__amdgpu_buffer_rsrc_t rs, int off) { return __builtin_amdgcn_raw_buffer_load_b128(rs, off, 0, 16); }
__device__ __forceinline__ void st_unit(__amdgpu_buffer_rsrc_t rs, int off, u32 a, u32 b, u32 tag) {
  __builtin_amdgcn_raw_buffer_store_b128(u32x4{a, tag, b, tag}, rs, off, 0, 16);
}
__device__ __forceinline__ bool tags_ok(u32x4 v, u32 tag) { return v.y == tag && v.w == tag; }
constexpr int OOB = 0x7ffffff0;
template <int N> __device__ __forceinline__ void wait_units(__amdgpu_buffer_rsrc_t rs, const int (&off)[N], u32x4 (&v)[N], u32 tag) {
  for (int spin = 0; spin < SPIN_CAP; ++spin) {
    bool ok[N], all = true;
#pragma unroll
    for (int j = 0; j < N; ++j) { ok[j] = off[j] == OOB || tags_ok(v[j], tag); all = all && ok[j]; }
    if (__all(all)) break;
    __builtin_amdgcn_s_sleep(1);
    MEMFENCE();
    u32x4 nv[N];
#pragma unroll
    for (int j = 0; j < N; ++j) nv[j] = ld_unit(rs, ok[j] ? OOB : off[j]);
#pragma unroll
    for (int j = 0; j < N; ++j) v[j] = ok[j] ? v[j] : nv[j];
  }
}
__device__ __forceinline__ void pair_rows(u32x4 a, u32x4 b, u32 (&o)[8]) {
#pragma unroll
  for (int k = 0; k < 4; ++k) { o[2 * k] = __builtin_amdgcn_perm(b[k], a[k], 0x05040100u); o[2 * k + 1] = __builtin_amdgcn_perm(b[k], a[k], 0x07060302u); }
}
#define STAMP(i) do {} while (0)
#define STAMPW(i) do {} while (0)

struct Lds {
  f32x2 pr[8][64];
  f32x2 pa[8][64];
  f32x2 ml[8];
  f32x2 red[8][16];
  f32x2 redS[8][16];
  f32x2 st[8];
  float xmean, pad0[3];
  unsigned char pad[90 * 1024];
};

struct Att { float m, l, acc[8]; };

__device__ __forceinline__ void qk_dots(const u32x4 (&k)[4], const u32 (&q)[4], float (&d)[4]) {
#pragma unroll
  for (int i = 0; i < 4; ++i) { float a = dot2(k[i][0], q[0], 0.f); a = dot2(k[i][1], q[1], a); a = dot2(k[i][2], q[2], a); d[i] = dot2(k[i][3], q[3], a); }
}
__device__ __forceinline__ float soft_step(const float (&d)[4], Att& st, bool b8, bool b4) {
  const float e0 = (b8 ? d[1] : d[0]) + dpp<ROR8>(b8 ? d[0] : d[1]);
  const float e1 = (b8 ? d[3] : d[2]) + dpp<ROR8>(b8 ? d[2] : d[3]);
  float s = (b4 ? e1 : e0) + dpp<HMIR>(b4 ? e0 : e1);
  s += dpp<QX1>(s); s += dpp<QX2>(s);
  s = rbf(rbf(s) * SCALE);
  float mx = fmaxf(s, dpp<ROR4>(s)); mx = fmaxf(mx, dpp<ROR8>(mx));
  const float mn = fmaxf(st.m, mx);
  const float alpha = __builtin_amdgcn_exp2f((st.m - mn) * L2E);
  const float p = __builtin_amdgcn_exp2f((s - mn) * L2E);
  st.m = mn; st.l = st.l * alpha + p;
#pragma unroll
  for (int j = 0; j < 8; ++j) st.acc[j] *= alpha;
  return p;
}
__device__ __forceinline__ void pv_acc(const u32x4 (&v)[4], float p, Att& st) {
  const float p0 = dpp<NBC + 0>(p), p1 = dpp<NBC + 8>(p), p2 = dpp<NBC + 4>(p), p3 = dpp<NBC + 12>(p);
  const float pp[4] = {p0, p1, p2, p3};
#pragma unroll
  for (int i = 0; i < 4; ++i)
#pragma unroll
    for (int k = 0; k < 4; ++k) { st.acc[2 * k] = fmaf(pp[i], lo16(v[i][k]), st.acc[2 * k]); st.acc[2 * k + 1] = fmaf(pp[i], hi16(v[i][k]), st.acc[2 * k + 1]); }
}

__global__ void __launch_bounds__(NTHR, 2) fused_block(Params p) {
  __shared__ Lds L;
  const int tid = threadIdx.x, lane = tid & 63, wave = __builtin_amdgcn_readfirstlane(tid >> 6);
  const int b = blockIdx.x, h = b >> 3, s = b & 7;
  const int r = lane >> 4, c = lane & 15;
  const __amdgpu_buffer_rsrc_t rs = __builtin_amdgcn_make_buffer_rsrc(p.ws, 0, WS_END, 0x00020000);
  STAMP(0);
  const u32 ep_raw = __hip_atomic_load((GAS u32*)(p.ws + WS_EPOCH), __ATOMIC_RELAXED, __HIP_MEMORY_SCOPE_AGENT);
  const u32* xw = reinterpret_cast<const u32*>(p.x);
  u32 xa[5], xp[2]; u32x4 wqv[4];
  if (wave < 5) {
#pragma unroll
    for (int j = 0; j < 5; ++j) xa[j] = xw[lane + 64 * j];
#pragma unroll
    for (int m = 0; m < 2; ++m) xp[m] = xw[40 * s + 4 * (2 * wave + m) + r];
#pragma unroll
    for (int m = 0; m < 2; ++m) { const int pp = 40 * s + 4 * (2 * wave + m) + r;
      wqv[2 * m] = ld_nt(p.wq + (size_t)(2 * pp) * HD + h * DH + 8 * c); wqv[2 * m + 1] = ld_nt(p.wq + (size_t)(2 * pp + 1) * HD + h * DH + 8 * c); }
  }
  SCHED();
  __builtin_amdgcn_s_barrier();
  SCHED();
  const u16* kb = p.K + ((size_t)h * SK + s * 2048 + wave * 16 + r) * DH + 8 * c;
  const u16* vb = p.V + ((size_t)h * SK + s * 2048 + wave * 16 + r) * DH + 8 * c;
  constexpr int TSTR = 128 * DH;
  u32x4 bk[DEPTH][4], bv[DEPTH][4];
#pragma unroll
  for (int i = 0; i < 4; ++i) bk[0][i] = ld_nt(kb + i * 4 * DH);
  SCHED();
  const u32 tag = __builtin_amdgcn_readfirstlane(ep_raw) + 1u;
  STAMP(20);
  if (wave < 5) {
    const float c0 = lo16(__builtin_amdgcn_readfirstlane(xa[0]));
    float s1 = 0.f, s2 = 0.f;
#pragma unroll
    for (int j = 0; j < 5; ++j) { const float a = lo16(xa[j]) - c0, bb = hi16(xa[j]) - c0; s1 += a + bb; s2 = fmaf(a, a, fmaf(bb, bb, s2)); }
    s1 = wave_sum(s1) * (1.f / D); s2 = wave_sum(s2) * (1.f / D);
    const float meanb = rbf(c0 + s1), rden = frcp(rbf(__builtin_amdgcn_sqrtf(rbf(rbf(fmaxf(s2 - s1 * s1, 0.f)) + EPS))));
    STAMP(21);
    if (tid == 0) L.xmean = meanb;
    float a[8] = {0.f, 0.f, 0.f, 0.f, 0.f, 0.f, 0.f, 0.f};
#pragma unroll
    for (int m = 0; m < 2; ++m) {
      const u32 xn = cvtpk(rbf(lo16(xp[m]) - meanb) * rden, rbf(hi16(xp[m]) - meanb) * rden);
      u32 wp[8]; pair_rows(wqv[2 * m], wqv[2 * m + 1], wp);
#pragma unroll
      for (int j = 0; j < 8; ++j) a[j] = dot2(wp[j], xn, a[j]);
    }
    L.pr[wave][4 * c + r] = fold8_rows(a);
    STAMP(22);
  }
  __syncthreads();
  STAMP(23);
  if (wave == 0) {
    f32x2 q = L.pr[0][lane];
#pragma unroll
    for (int w = 1; w < 5; ++w) q += L.pr[w][lane];
    st_unit(rs, WS_QP + b * 1024 + lane * 16, asu(q.x), asu(q.y), tag);
  }
  STAMP(1);
  const int qoff[1] = {WS_QP + (8 * h + wave) * 1024 + lane * 16};
  u32x4 qu[1] = {ld_unit(rs, qoff[0])};
  SCHED();
#pragma unroll
  for (int i = 0; i < 4; ++i) bv[0][i] = ld_nt(vb + i * 4 * DH);
#pragma unroll
  for (int d = 1; d < DEPTH; ++d) {
#pragma unroll
    for (int i = 0; i < 4; ++i) bk[d][i] = ld_nt(kb + d * TSTR + i * 4 * DH);
#pragma unroll
    for (int i = 0; i < 4; ++i) bv[d][i] = ld_nt(vb + d * TSTR + i * 4 * DH); }
  SCHED();
  wait_units<1>(rs, qoff, qu, tag);
  STAMP(24);
  L.pa[wave][lane] = f32x2{asf(qu[0].x), asf(qu[0].z)};
  __syncthreads();
  u32 q[4];
  {
    f32x4 a = {0.f, 0.f, 0.f, 0.f}, bb = a;
#pragma unroll
    for (int w = 0; w < 8; ++w) { a += *reinterpret_cast<const f32x4*>(&L.pa[w][4 * c]); bb += *reinterpret_cast<const f32x4*>(&L.pa[w][4 * c + 2]); }
    q[0] = cvtpk(a.x, a.y); q[1] = cvtpk(a.z, a.w); q[2] = cvtpk(bb.x, bb.y); q[3] = cvtpk(bb.z, bb.w);
  }
  const float xpivot = L.xmean;
  STAMP(2);
  Att st; st.m = -INFINITY; st.l = 0.f;
#pragma unroll
  for (int j = 0; j < 8; ++j) st.acc[j] = 0.f;
  const bool b8 = c & 8, b4 = c & 4;
#pragma unroll 1
  for (int i = 0; i < TPW - DEPTH; i += DEPTH) {
#pragma unroll
    for (int d = 0; d < DEPTH; ++d) {
      float dd[4]; qk_dots(bk[d], q, dd);
      asm volatile("" : "+v"(dd[0]), "+v"(dd[1]), "+v"(dd[2]), "+v"(dd[3]));
      SCHED();
#pragma unroll
      for (int j = 0; j < 4; ++j) bk[d][j] = ld_nt(kb + (size_t)(i + d + DEPTH) * TSTR + j * 4 * DH);
      SCHED();
      const float pv = soft_step(dd, st, b8, b4);
      pv_acc(bv[d], pv, st);
      asm volatile("" : "+v"(st.acc[0]), "+v"(st.acc[1]), "+v"(st.acc[2]), "+v"(st.acc[3]), "+v"(st.acc[4]), "+v"(st.acc[5]), "+v"(st.acc[6]), "+v"(st.acc[7]));
      SCHED();
#pragma unroll
      for (int j = 0; j < 4; ++j) bv[d][j] = ld_nt(vb + (size_t)(i + d + DEPTH) * TSTR + j * 4 * DH);
      SCHED();
    }
  }
  const bool roleW = b < 160;
  const int kblk = b & 7, nblk = b >> 3;
  const int jf = 31 - (b >> 3), lgf = (b & 7) + 8 * (jf >> 2); const bool roleF = jf < 16 && lgf < 27; const int fb = 4 * lgf + (jf & 3);
  const int lgd = (b & 7) + 8 * (b >> 5); const bool roleD = (b >> 3) < 8 && lgd < 10; const int cb = 4 * lgd + ((b >> 3) & 3);
  u32x4 wW[4], wF[6], wD[8];
  u32 xF[3] = {0u, 0u, 0u};
  const int dW = 2 * (8 * wave + ((lane >> 2) & 7)) + (lane >> 5);
  if (roleW) {
#pragma unroll
    for (int i = 0; i < 4; ++i) wW[i] = ld_w(p.wo + (size_t)(512 * kblk + 128 * i + dW) * D + 32 * nblk + 8 * (lane & 3));
  }
  if (roleF) {
    const u16* wm = (lane & 2) ? p.wu : p.wg;
#pragma unroll
    for (int j = 0; j < 3; ++j) { int pp = (lane >> 2) + 16 * wave + 128 * j; pp = pp < 320 ? pp : 319;
      xF[j] = xw[pp];
      wF[2 * j] = ld_w(wm + (size_t)(2 * pp) * F + 16 * fb + 8 * (lane & 1)); wF[2 * j + 1] = ld_w(wm + (size_t)(2 * pp + 1) * F + 16 * fb + 8 * (lane & 1)); }
  }
  SCHED();
#pragma unroll
  for (int d = 0; d < DEPTH; ++d) {
    float dd[4]; qk_dots(bk[d], q, dd);
    const float pv = soft_step(dd, st, b8, b4);
    pv_acc(bv[d], pv, st);
  }
  STAMPW(8);
  {
    const float mw = max32(max16(st.m));
    const float f = fexp2((st.m - mw) * L2E);
    float l = st.l * f; l += dpp<ROR4>(l); l += dpp<ROR8>(l); l = fold16(l, l); l = fold32(l, l);
#pragma unroll
    for (int j = 0; j < 8; ++j) st.acc[j] *= f;
    L.pr[wave][4 * c + r] = fold8_rows(st.acc);
    if (lane == 0) L.ml[wave] = f32x2{mw, l};
  }
  __syncthreads();
  if (wave == 0) {
    const f32x2 ml = L.ml[lane & 7];
    f32x2 pa[8];
#pragma unroll
    for (int w = 0; w < 8; ++w) pa[w] = L.pr[w][lane];
    float M = fmaxf(ml.x, dpp<QX1>(ml.x)); M = fmaxf(M, dpp<QX2>(M)); M = fmaxf(M, dpp<HMIR>(M));
    const float f = fexp2((ml.x - M) * L2E);
    float l = f * ml.y; l += dpp<QX1>(l); l += dpp<QX2>(l); l += dpp<HMIR>(l);
    f32x2 o = dpp<NBC + 0>(f) * pa[0];
    o += dpp<NBC + 1>(f) * pa[1]; o += dpp<NBC + 2>(f) * pa[2]; o += dpp<NBC + 3>(f) * pa[3];
    o += dpp<NBC + 4>(f) * pa[4]; o += dpp<NBC + 5>(f) * pa[5]; o += dpp<NBC + 6>(f) * pa[6]; o += dpp<NBC + 7>(f) * pa[7];
    st_unit(rs, WS_AP + b * 1024 + lane * 16, asu(o.x), asu(o.y), tag);
    if (lane == 0) st_unit(rs, WS_ML + b * 16, asu(M), asu(l), tag);
  }
  STAMP(3);
  if (roleW) {
    const int i = lane & 3, qa = (lane >> 2) & 3, dp = 8 * wave + ((lane >> 2) & 7), sh = lane >> 5, hh = 4 * kblk + i;
    int off[5]; u32x4 a[5];
    off[0] = WS_ML + (hh * 8 + 4 * sh + qa) * 16;
#pragma unroll
    for (int k = 0; k < 4; ++k) off[1 + k] = WS_AP + (hh * 8 + 4 * sh + ((qa - k) & 3)) * 1024 + dp * 16;
#pragma unroll
    for (int k = 0; k < 5; ++k) a[k] = ld_unit(rs, off[k]);
    SCHED();
    u32 wp01[8], wp23[8]; pair_rows(wW[0], wW[1], wp01); pair_rows(wW[2], wW[3], wp23);
    wait_units<5>(rs, off, a, tag);
    STAMP(4);
    const float m = asf(a[0].x);
    float M = fmaxf(m, dpp<ROR4>(m)); M = fmaxf(M, dpp<ROR8>(M)); M = max32(M);
    const float w = fexp2((m - M) * L2E);
    float l = w * asf(a[0].z); l += dpp<ROR4>(l); l += dpp<ROR8>(l); l = fold32(l, l);
    const float w1 = dpp<ROR4>(w), w2 = dpp<ROR8>(w), w3 = dpp<ROR4 + 8>(w);
    const float o0 = fmaf(w3, asf(a[4].x), fmaf(w2, asf(a[3].x), fmaf(w1, asf(a[2].x), w * asf(a[1].x))));
    const float o1 = fmaf(w3, asf(a[4].z), fmaf(w2, asf(a[3].z), fmaf(w1, asf(a[2].z), w * asf(a[1].z))));
    const float o = fold32(o0, o1) * frcp(l);
    const u32 p01 = cvtpk(dpp<0x00>(o), dpp<0x55>(o)), p23 = cvtpk(dpp<0xAA>(o), dpp<0xFF>(o));
    float y[8];
#pragma unroll
    for (int j = 0; j < 8; ++j) y[j] = dot2(wp23[j], p23, dot2(wp01[j], p01, 0.f));
    f32x2 t = fold8_rows(y);
    t.x += dpp<ROR8>(t.x); t.y += dpp<ROR8>(t.y); t.x += dpp<ROR4>(t.x); t.y += dpp<ROR4>(t.y);
    if (c < 4) L.red[wave][4 * c + r] = t;
    __syncthreads();
    if (wave == 0) {
      f32x2 z = L.red[2 * r][c] + L.red[2 * r + 1][c];
      z.x = fold16(z.x, z.x); z.y = fold16(z.y, z.y); z.x = fold32(z.x, z.x); z.y = fold32(z.y, z.y);
      if (lane < 16) st_unit(rs, WS_YP + (kblk * 320 + 16 * nblk + lane) * 16, asu(z.x), asu(z.y), tag);
    }
    STAMP(5);
  }
  if (roleF) {
    const int cmb = lane & 3; const bool has2 = wave < 4;
    int off[6]; u32x4 y[6];
#pragma unroll
    for (int j = 0; j < 3; ++j) { const int pp = (lane >> 2) + 16 * wave + 128 * j;
      off[2 * j] = (j < 2 || has2) ? WS_YP + ((2 * cmb) * 320 + pp) * 16 : OOB; off[2 * j + 1] = (j < 2 || has2) ? WS_YP + ((2 * cmb + 1) * 320 + pp) * 16 : OOB; }
#pragma unroll
    for (int k = 0; k < 6; ++k) y[k] = ld_unit(rs, off[k]);
    SCHED();
    u32 wp[3][8];
#pragma unroll
    for (int j = 0; j < 3; ++j) pair_rows(wF[2 * j], wF[2 * j + 1], wp[j]);
    {
      float cs[8];
#pragma unroll
      for (int k = 0; k < 8; ++k) { cs[k] = dot2(wp[1][k], 0x3f803f80u, dot2(wp[0][k], 0x3f803f80u, 0.f)); cs[k] = dot2(wp[2][k], has2 ? 0x3f803f80u : 0u, cs[k]); }
      f32x2 t = fold8_rows(cs);
      t.x += dpp<ROR8>(t.x); t.y += dpp<ROR8>(t.y); t.x += dpp<ROR4>(t.x); t.y += dpp<ROR4>(t.y);
      if (c < 4) L.redS[wave][4 * c + r] = t;
    }
    wait_units<6>(rs, off, y, tag);
    STAMP(6);
    float g[8] = {0.f, 0.f, 0.f, 0.f, 0.f, 0.f, 0.f, 0.f};
    float s1 = 0.f, s2 = 0.f;
#pragma unroll
    for (int j = 0; j < 3; ++j) {
      float a0 = asf(y[2 * j].x) + asf(y[2 * j + 1].x), a1 = asf(y[2 * j].z) + asf(y[2 * j + 1].z);
      a0 += dpp<QX1>(a0); a1 += dpp<QX1>(a1); a0 += dpp<QX2>(a0); a1 += dpp<QX2>(a1);
      u32 hp = cvtpk(lo16(xF[j]) + rbf(a0), hi16(xF[j]) + rbf(a1));
      const bool valid = j < 2 || has2;
      hp = valid ? hp : 0u;
      const float h0 = valid ? lo16(hp) - xpivot : 0.f, h1 = valid ? hi16(hp) - xpivot : 0.f;
      s1 += h0 + h1; s2 = fmaf(h0, h0, fmaf(h1, h1, s2));
#pragma unroll
      for (int k = 0; k < 8; ++k) g[k] = dot2(wp[j][k], hp, g[k]);
    }
    s1 = quads_sum(s1); s2 = quads_sum(s2);
    f32x2 t = fold8_rows(g);
    t.x += dpp<ROR8>(t.x); t.y += dpp<ROR8>(t.y); t.x += dpp<ROR4>(t.x); t.y += dpp<ROR4>(t.y);
    if (c < 4) L.red[wave][4 * c + r] = t;
    if (lane == 0) L.st[wave] = f32x2{s1, s2};
    __syncthreads();
    if (wave == 0) {
      f32x2 A = L.red[2 * r][c] + L.red[2 * r + 1][c], S = L.redS[2 * r][c] + L.redS[2 * r + 1][c];
      f32x2 sv = L.st[lane & 7];
      A.x = fold16(A.x, A.x); A.y = fold16(A.y, A.y); S.x = fold16(S.x, S.x); S.y = fold16(S.y, S.y);
      A.x = fold32(A.x, A.x); A.y = fold32(A.y, A.y); S.x = fold32(S.x, S.x); S.y = fold32(S.y, S.y);
      sv.x += dpp<QX1>(sv.x); sv.y += dpp<QX1>(sv.y); sv.x += dpp<QX2>(sv.x); sv.y += dpp<QX2>(sv.y); sv.x += dpp<HMIR>(sv.x); sv.y += dpp<HMIR>(sv.y);
      const float e1 = sv.x * (1.f / D), e2 = sv.y * (1.f / D);
      const float meanb = rbf(xpivot + e1), rden = frcp(rbf(__builtin_amdgcn_sqrtf(rbf(rbf(fmaxf(e2 - e1 * e1, 0.f)) + EPS))));
      const float v0 = rbf((A.x - meanb * S.x) * rden), v1 = rbf((A.y - meanb * S.y) * rden);
      const float u0 = dpp<ROR8>(v0), u1 = dpp<ROR8>(v1);
      const float s0 = rbf(v0 * frcp(1.f + fexp2(-v0 * L2E))), sg1 = rbf(v1 * frcp(1.f + fexp2(-v1 * L2E)));
      const u32 ap = cvtpk(s0 * u0, sg1 * u1);
      const u32 nx = asu(dpp<QX1>(asf(ap)));
      if (lane < 8 && !(lane & 1)) st_unit(rs, WS_AC + (4 * fb + (lane >> 1)) * 16, ap, nx, tag);
    }
    STAMP(7);
  }
  if (roleD) {
#pragma unroll
    for (int j = 0; j < 4; ++j) { int pp = (lane >> 1) + 32 * wave + 256 * j; pp = pp < 864 ? pp : 863;
      wD[2 * j] = ld_w(p.wd + (size_t)(2 * pp) * D + 16 * cb + 8 * (lane & 1)); wD[2 * j + 1] = ld_w(p.wd + (size_t)(2 * pp + 1) * D + 16 * cb + 8 * (lane & 1)); }
    const u32 xres = xw[8 * cb + (lane & 7)];
    SCHED();
    u32 hres = 0;
    if (wave == 7) {
      int off[1]; u32x4 y[1];
      off[0] = WS_YP + ((lane >> 3) * 320 + 8 * cb + (lane & 7)) * 16;
      y[0] = ld_unit(rs, off[0]);
      wait_units<1>(rs, off, y, tag);
      float a0 = asf(y[0].x), a1 = asf(y[0].z);
      a0 += dpp<ROR8>(a0); a1 += dpp<ROR8>(a1); a0 = fold16(a0, a0); a1 = fold16(a1, a1); a0 = fold32(a0, a0); a1 = fold32(a1, a1);
      hres = cvtpk(lo16(xres) + rbf(a0), hi16(xres) + rbf(a1));
    }
    const bool has3 = wave < 3;
    int off[4]; u32x4 a[4];
#pragma unroll
    for (int j = 0; j < 4; ++j) { const int pp = (lane >> 1) + 32 * wave + 256 * j; off[j] = (j < 3 || has3) ? WS_AC + (pp >> 1) * 16 : OOB; }
#pragma unroll
    for (int j = 0; j < 4; ++j) a[j] = ld_unit(rs, off[j]);
    SCHED();
    u32 wp[4][8];
#pragma unroll
    for (int j = 0; j < 4; ++j) pair_rows(wD[2 * j], wD[2 * j + 1], wp[j]);
    wait_units<4>(rs, off, a, tag);
    STAMP(16);
    float o[8] = {0.f, 0.f, 0.f, 0.f, 0.f, 0.f, 0.f, 0.f};
#pragma unroll
    for (int j = 0; j < 4; ++j) {
      u32 av = (lane & 2) ? a[j].z : a[j].x; av = (j < 3 || has3) ? av : 0u;
#pragma unroll
      for (int k = 0; k < 8; ++k) o[k] = dot2(wp[j][k], av, o[k]);
    }
    f32x2 t = fold8_rows(o);
    t.x += dpp<ROR8>(t.x); t.y += dpp<ROR8>(t.y); t.x += dpp<ROR4>(t.x); t.y += dpp<ROR4>(t.y); t.x += dpp<QX2>(t.x); t.y += dpp<QX2>(t.y);
    if (c < 2) L.red[wave][4 * c + r] = t;
    __syncthreads();
    if (wave == 7) {
      f32x2 z = L.red[lane >> 3][lane & 7];
      z.x += dpp<ROR8>(z.x); z.y += dpp<ROR8>(z.y); z.x = fold16(z.x, z.x); z.y = fold16(z.y, z.y); z.x = fold32(z.x, z.x); z.y = fold32(z.y, z.y);
      const u32 ov = cvtpk(lo16(hres) + rbf(z.x), hi16(hres) + rbf(z.y));
      if (lane < 8) reinterpret_cast<u32*>(p.out)[8 * cb + lane] = ov;
      if (b == 0 && lane == 0) __hip_atomic_store((GAS u32*)(p.ws + WS_EPOCH), tag, __ATOMIC_RELAXED, __HIP_MEMORY_SCOPE_AGENT);
    }
    STAMP(17);
  }
}

extern "C" void kernel_launch(void* const* d_in, const int* in_sizes, int n_in, void* d_out, int out_size, void* d_ws, size_t ws_size,
                              hipStream_t stream) {
  Params p;
  p.x = (const u16*)d_in[0]; p.K = (const u16*)d_in[1]; p.V = (const u16*)d_in[2]; p.wq = (const u16*)d_in[3]; p.wo = (const u16*)d_in[4];
  p.wg = (const u16*)d_in[6]; p.wu = (const u16*)d_in[7]; p.wd = (const u16*)d_in[8];
  p.out = (u16*)d_out; p.ws = (unsigned char*)d_ws;
  fused_block<<<NBLK, NTHR, 0, stream>>>(p);
}
```

```cpp
#include <hip/hip_runtime.h>
#include <hip/hip_bf16.h>
#include <stdint.h>
#include <type_traits>

typedef unsigned u32;
typedef unsigned long long u64;
typedef unsigned short u16;
typedef u32 u32x4 __attribute__((ext_vector_type(4)));
typedef u32 u32x2 __attribute__((ext_vector_type(2)));
typedef float f32x2 __attribute__((ext_vector_type(2)));
typedef __bf16 bf16x2 __attribute__((ext_vector_type(2)));
#define GAS __attribute__((address_space(1)))

constexpr int D = 640, H = 32, SK = 16384, DH = 128, F = 1728, HD = H * DH;
constexpr int NBLK = 256, NTHR = 512;
constexpr int NR = 16;
constexpr int WIN = 2;
constexpr int RING = 3;
constexpr u32 PACE_DT = 1760;
constexpr u32 START_OFF = 60;
constexpr float L2E = 1.4426950408889634f;
constexpr float SCALE = 0.08838834764831845f;
constexpr float EPS = 1e-5f;
constexpr int WS_EPOCH = 0;
constexpr int WS_QP = 4096;
constexpr int WS_AP = WS_QP + 256 * 1024;
constexpr int WS_ML = WS_AP + 256 * 1024;
constexpr int WS_YP = WS_ML + 4096;
constexpr int WS_AC = WS_YP + 8 * 320 * 16;
constexpr int WS_END = WS_AC + 2 * 432 * 16;
constexpr int SPIN_CAP = 1 << 18;

struct Params {
  const u16 *x, *K, *V, *wq, *wo, *wg, *wu, *wd;
  u16* out;
  unsigned char* ws;
};

template <int I, int N, class Fn> __device__ __forceinline__ void static_for(Fn&& f) { if constexpr (I < N) { f(std::integral_constant<int, I>{}); static_for<I + 1, N>(f); } }
#define SCHED() __builtin_amdgcn_sched_barrier(0)
#define MEMFENCE() asm volatile("" ::: "memory")
#define PIN8(a) asm volatile("" : "+v"(a[0]), "+v"(a[1]), "+v"(a[2]), "+v"(a[3]), "+v"(a[4]), "+v"(a[5]), "+v"(a[6]), "+v"(a[7]))
__device__ __forceinline__ float asf(u32 x) { return __builtin_bit_cast(float, x); }
__device__ __forceinline__ u32 asu(float x) { return __builtin_bit_cast(u32, x); }
__device__ __forceinline__ float dot2(u32 a, u32 b, float c) {
  return __builtin_amdgcn_fdot2_f32_bf16(__builtin_bit_cast(bf16x2, a), __builtin_bit_cast(bf16x2, b), c, false);
}
__device__ __forceinline__ u32 cvtpk(float lo, float hi) { bf16x2 v = {(__bf16)lo, (__bf16)hi}; return __builtin_bit_cast(u32, v); }
__device__ __forceinline__ float rbf(float x) { return asf(cvtpk(x, x) << 16); }
__device__ __forceinline__ float lo16(u32 v) { return asf(v << 16); }
__device__ __forceinline__ float hi16(u32 v) { return asf(v & 0xffff0000u); }
__device__ __forceinline__ float frcp(float x) { return __builtin_amdgcn_rcpf(x); }
__device__ __forceinline__ float fexp2(float x) { return __builtin_amdgcn_exp2f(x); }
template <int CTRL> __device__ __forceinline__ float dpp(float x) {
  return asf((u32)__builtin_amdgcn_mov_dpp((int)asu(x), CTRL, 0xf, 0xf, true));
}
constexpr int QX1 = 0xB1, QX2 = 0x4E, HMIR = 0x141, ROR4 = 0x124, ROR8 = 0x128, NBC = 0x150;
__device__ __forceinline__ float fold32(float a, float b) {
  auto r = __builtin_amdgcn_permlane32_swap(asu(a), asu(b), false, false); return asf(r[0]) + asf(r[1]); }
__device__ __forceinline__ float fold16(float a, float b) {
  auto r = __builtin_amdgcn_permlane16_swap(asu(a), asu(b), false, false); return asf(r[0]) + asf(r[1]); }
__device__ __forceinline__ float max32(float a) {
  auto r = __builtin_amdgcn_permlane32_swap(asu(a), asu(a), false, false); return fmaxf(asf(r[0]), asf(r[1])); }
__device__ __forceinline__ float max16(float a) {
  auto r = __builtin_amdgcn_permlane16_swap(asu(a), asu(a), false, false); return fmaxf(asf(r[0]), asf(r[1])); }
__device__ __forceinline__ float wave_sum(float v) {
  v += dpp<QX1>(v); v += dpp<QX2>(v); v += dpp<HMIR>(v); v += dpp<ROR8>(v);
  v = fold16(v, v); return fold32(v, v);
}
__device__ __forceinline__ f32x2 fold8_rows(const float (&a)[8]) {
  float t0 = fold32(a[0], a[4]), t1 = fold32(a[1], a[5]), t2 = fold32(a[2], a[6]), t3 = fold32(a[3], a[7]);
  return f32x2{fold16(t0, t2), fold16(t1, t3)};
}
__device__ __forceinline__ void pair_rows(u32x4 a, u32x4 b, u32 (&o)[8]) {
#pragma unroll
  for (int k = 0; k < 4; ++k) { o[2 * k] = __builtin_amdgcn_perm(b[k], a[k], 0x05040100u); o[2 * k + 1] = __builtin_amdgcn_perm(b[k], a[k], 0x07060302u); }
}
__device__ __forceinline__ u32x4 ld_nt(const u16* p) { return __builtin_nontemporal_load(reinterpret_cast<const u32x4*>(p)); }

__device__ __forceinline__ u32x4 ld_unit(__amdgpu_buffer_rsrc_t rs, int off) { return __builtin_amdgcn_raw_buffer_load_b128(rs, off, 0, 16); }
__device__ __forceinline__ void st_unit(__amdgpu_buffer_rsrc_t rs, int off, u32 a, u32 b, u32 tag) {
  __builtin_amdgcn_raw_buffer_store_b128(u32x4{a, tag, b, tag}, rs, off, 0, 16);
}
template <int N, int SLEEP> __device__ __forceinline__ void wait_units(__amdgpu_buffer_rsrc_t rs, const int (&off)[N], u32x4 (&v)[N], u32 tag) {
#pragma nounroll
  for (int spin = SPIN_CAP; spin > 0; --spin) {
    u32 bad = 0u;
#pragma unroll
    for (int j = 0; j < N; ++j) bad |= (v[j].y ^ tag) | (v[j].w ^ tag);
    if (__builtin_amdgcn_ballot_w64(bad != 0u) == 0ull) break;
    __builtin_amdgcn_s_sleep(SLEEP);
    MEMFENCE();
#pragma unroll
    for (int j = 0; j < N; ++j) v[j] = ld_unit(rs, off[j]);
  }
}
__device__ __forceinline__ void watch_units(__amdgpu_buffer_rsrc_t rs, int off, u32 tag) {
#pragma nounroll
  for (int spin = SPIN_CAP; spin > 0; --spin) {
    const u32x4 v = ld_unit(rs, off);
    if (__builtin_amdgcn_ballot_w64(((v.y ^ tag) | (v.w ^ tag)) != 0u) == 0ull) break;
    __builtin_amdgcn_s_sleep(4);
    MEMFENCE();
  }
}
#define UL(r0, r3, o) "global_load_dwordx4 v[" #r0 ":" #r3 "], %[" #o "], %[base] sc1\n\t"
#define UCHK(r0, r1, r3, o, l) \
    "v_cmp_ne_u32 vcc, %[tag], v" #r1 "\n\tv_cmp_ne_u32 %[tmp], %[tag], v" #r3 "\n\ts_or_b64 vcc, vcc, %[tmp]\n\t" \
    "s_cbranch_scc0 " #l "f\n\t" UL(r0, r3, o) "s_mov_b32 %[any], 1\n\t" #l ":\n\t"
struct Units5 { u32x4 a0, a1, a2, a3, a4; };
__device__ __forceinline__ void gather_units5(const unsigned char* ws, const int (&off)[5], Units5& u, u32 tag) {
  int spin = SPIN_CAP; u32 any; u64 tmp;
  asm volatile(UL(232, 235, o0) UL(236, 239, o1) UL(240, 243, o2) UL(244, 247, o3) UL(248, 251, o4)
               "1:\n\ts_waitcnt vmcnt(0)\n\ts_mov_b32 %[any], 0\n\t"
               UCHK(232, 233, 235, o0, 3) UCHK(236, 237, 239, o1, 4) UCHK(240, 241, 243, o2, 5) UCHK(244, 245, 247, o3, 6) UCHK(248, 249, 251, o4, 7)
               "s_cmp_eq_u32 %[any], 0\n\ts_cbranch_scc1 9f\n\ts_sub_u32 %[spin], %[spin], 1\n\ts_cmp_eq_u32 %[spin], 0\n\ts_cbranch_scc1 8f\n\t"
               "s_sleep 1\n\ts_branch 1b\n\t8:\n\ts_waitcnt vmcnt(0)\n\t9:"
               : "=&{v[232:235]}"(u.a0), "=&{v[236:239]}"(u.a1), "=&{v[240:243]}"(u.a2), "=&{v[244:247]}"(u.a3), "=&{v[248:251]}"(u.a4),
                 [spin] "+s"(spin), [any] "=&s"(any), [tmp] "=&s"(tmp)
               : [o0] "v"(off[0]), [o1] "v"(off[1]), [o2] "v"(off[2]), [o3] "v"(off[3]), [o4] "v"(off[4]), [base] "s"(ws), [tag] "s"(tag)
               : "memory", "vcc", "scc");
}

struct Lds {
  f32x2 pr[8][64];
  u32 q[64];
  f32x2 ml[8];
  f32x2 red[8][32];
  f32x2 redS[8][32];
  u32 hp[320];
  u32 act[864];
  f32x2 st[8];
  float xmean; u32 tag, pad0[2];
};

__device__ __forceinline__ void qk_dots(const u32x4 (&k)[4], const u32 (&q)[4], float (&d)[4]) {
#pragma unroll
  for (int i = 0; i < 4; ++i) { float a = dot2(k[i][0], q[0], 0.f); a = dot2(k[i][1], q[1], a); a = dot2(k[i][2], q[2], a); d[i] = dot2(k[i][3], q[3], a); }
}
__device__ __forceinline__ float tile_score(const float (&d)[4], bool b8, bool b4) {
  const float e0 = (b8 ? d[1] : d[0]) + dpp<ROR8>(b8 ? d[0] : d[1]);
  const float e1 = (b8 ? d[3] : d[2]) + dpp<ROR8>(b8 ? d[2] : d[3]);
  float s = (b4 ? e1 : e0) + dpp<HMIR>(b4 ? e0 : e1);
  s += dpp<QX1>(s); s += dpp<QX2>(s);
  return rbf(rbf(s) * SCALE);
}
__device__ __forceinline__ void pv_acc(const u32x4 (&v)[4], float p, float (&acc)[8]) {
  const float pp[4] = {dpp<NBC + 0>(p), dpp<NBC + 8>(p), dpp<NBC + 4>(p), dpp<NBC + 12>(p)};
#pragma unroll
  for (int i = 0; i < 4; ++i)
#pragma unroll
    for (int k = 0; k < 4; ++k) { acc[2 * k] = fmaf(pp[i], lo16(v[i][k]), acc[2 * k]); acc[2 * k + 1] = fmaf(pp[i], hi16(v[i][k]), acc[2 * k + 1]); }
}

__global__ void __launch_bounds__(NTHR, 2) fused_block(Params p) {
  __shared__ Lds L;
  const int tid = threadIdx.x, lane = tid & 63, wave = __builtin_amdgcn_readfirstlane(tid >> 6);
  const int b = blockIdx.x;
  const int h = b >> 3, s = (b + h) & 7;
  const int r = lane >> 4, c = lane & 15;
  const __amdgpu_buffer_rsrc_t rs = __builtin_amdgcn_make_buffer_rsrc(p.ws, 0, WS_END, 0x00020000);
  const int xb = b & 7, tb = (b >> 3) - 20;
  const bool roleW = tb < 0;
  const int kblk = xb, nblk = b >> 3;
  const bool roleD = tb >= 0 && tb < (xb < 2 ? 8 : 4);
  const int cb = 4 * (xb + 8 * (tb >> 2)) + (tb & 3);
  const int fb = xb < 2 ? 2 * (24 + 2 * xb + ((tb - 8) >> 1)) + (tb & 1) : 2 * (4 * (xb - 2) + ((tb - 4) >> 1)) + (tb & 1);
  const bool roleF = tb >= 0 && !roleD && fb < 54;
  const int qa = (lane >> 2) & 3;

  const size_t kvoff = ((size_t)h * SK + s * 2048 + wave * 16 + r) * DH + 8 * c;
  u32x4 buf[RING][4];
#define LOAD_GROUP(g) do { const u16* gp_ = ((g) < NR ? p.K : p.V) + kvoff + (size_t)((g) % NR) * (128 * DH); \
    _Pragma("unroll") for (int j_ = 0; j_ < 4; ++j_) buf[(g) % RING][j_] = ld_nt(gp_ + j_ * 4 * DH); } while (0)
  u64 tpace0;
#define PACE(g) do { const u32 due_ = ((u32)(g) * PACE_DT + (u32)wave * (PACE_DT / 8)) >> 4; \
    while ((int)((u32)(__builtin_amdgcn_s_memrealtime() - tpace0) - due_) < 0) __builtin_amdgcn_s_sleep(1); } while (0)

  const u32 ep_raw = __hip_atomic_load((GAS u32*)(p.ws + WS_EPOCH), __ATOMIC_RELAXED, __HIP_MEMORY_SCOPE_AGENT);
  const u32* xw = reinterpret_cast<const u32*>(p.x);
  u32x4 wW[4];
  {
    const bool qw = wave < 5;
    u32 xa[5], xp[2]; u32x4 wqv[4];
#pragma unroll
    for (int j = 0; j < 5; ++j) xa[j] = xw[lane + 64 * j];
#pragma unroll
    for (int m = 0; m < 2; ++m) xp[m] = xw[qw ? 40 * s + 4 * (2 * wave + m) + r : 0];
#pragma unroll
    for (int m = 0; m < 2; ++m) { const int pp = 40 * s + 4 * (2 * wave + m) + r;
      wqv[2 * m] = ld_nt(qw ? p.wq + (size_t)(2 * pp) * HD + h * DH + 8 * c : p.wq);
      wqv[2 * m + 1] = ld_nt(qw ? p.wq + (size_t)(2 * pp + 1) * HD + h * DH + 8 * c : p.wq); }
    SCHED();
    asm volatile("" : "+v"(xa[0]), "+v"(xa[4]));
    const float c0 = lo16(__builtin_amdgcn_readfirstlane(xa[0]));
    float s1 = 0.f, s2 = 0.f;
#pragma unroll
    for (int j = 0; j < 5; ++j) { const float a = lo16(xa[j]) - c0, bb = hi16(xa[j]) - c0; s1 += a + bb; s2 = fmaf(a, a, fmaf(bb, bb, s2)); }
    s1 = wave_sum(s1) * (1.f / D); s2 = wave_sum(s2) * (1.f / D);
    const float meanb = rbf(c0 + s1), rden = frcp(rbf(__builtin_amdgcn_sqrtf(rbf(rbf(fmaxf(s2 - s1 * s1, 0.f)) + EPS))));
    float a[8] = {0.f, 0.f, 0.f, 0.f, 0.f, 0.f, 0.f, 0.f};
#pragma unroll
    for (int m = 0; m < 2; ++m) {
      const u32 xn = cvtpk(rbf(lo16(xp[m]) - meanb) * rden, rbf(hi16(xp[m]) - meanb) * rden);
      u32 wp[8]; pair_rows(wqv[2 * m], wqv[2 * m + 1], wp);
#pragma unroll
      for (int j = 0; j < 8; ++j) a[j] = dot2(wp[j], xn, a[j]);
    }
    f32x2 t = fold8_rows(a);
    u32 wmask = roleW ? ~0u : 0u;
    asm volatile("" : "+v"(t.x), "+v"(t.y), "+v"(wmask));
    SCHED();
#pragma unroll
    for (int k = 0; k < 4; ++k)
      wW[k] = ld_nt(p.wo + ((u32)((512 * kblk + 128 * qa + 2 * (8 * wave + 4 * ((lane >> 4) & 1) + k) + (lane >> 5)) * D + 32 * nblk + 8 * (lane & 3)) & wmask));
    SCHED();
    if (qw) L.pr[wave][4 * c + r] = t;
    if (tid == 0) { L.xmean = meanb; L.tag = ep_raw + 1u; }
  }
  __syncthreads();
  tpace0 = __builtin_amdgcn_s_memrealtime() + START_OFF;
  if (wave == 0) {
    const u32 tg = __builtin_amdgcn_readfirstlane(ep_raw) + 1u;
    f32x2 q = L.pr[0][lane];
#pragma unroll
    for (int w = 1; w < 5; ++w) q += L.pr[w][lane];
    st_unit(rs, WS_QP + (h * 8 + s) * 1024 + lane * 16, asu(q.x), asu(q.y), tg);
    int off[8]; u32x4 qu[8];
#pragma unroll
    for (int k = 0; k < 8; ++k) { off[k] = WS_QP + (8 * h + k) * 1024 + lane * 16; qu[k] = ld_unit(rs, off[k]); }
    wait_units<8, 1>(rs, off, qu, tg);
    float q0 = 0.f, q1 = 0.f;
#pragma unroll
    for (int k = 0; k < 8; ++k) { q0 += asf(qu[k].x); q1 += asf(qu[k].z); }
    L.q[lane] = cvtpk(q0, q1);
  }
  SCHED();
  static_for<0, RING>([&](auto G) { constexpr int g = G; PACE(g); LOAD_GROUP(g); SCHED(); });
  __syncthreads();
  const u32 tag = __builtin_amdgcn_readfirstlane(L.tag);
  u32 q[4];
  { const u32x4 t = *reinterpret_cast<const u32x4*>(&L.q[4 * c]); q[0] = t.x; q[1] = t.y; q[2] = t.z; q[3] = t.w; }
  const float xpivot = L.xmean;

  const bool b8 = c & 8, b4 = c & 4;
  float sc[NR];
  static_for<0, NR>([&](auto G) { constexpr int g = G;
    float dd[4]; qk_dots(buf[g % RING], q, dd);
    asm volatile("" : "+v"(dd[0]), "+v"(dd[1]), "+v"(dd[2]), "+v"(dd[3]));
    SCHED();
    if constexpr (g + WIN >= RING) { PACE(g + WIN); LOAD_GROUP(g + WIN); }
    SCHED();
    sc[g] = tile_score(dd, b8, b4);
  });
  float mw = sc[0];
#pragma unroll
  for (int g = 1; g < NR; ++g) mw = fmaxf(mw, sc[g]);
  mw = fmaxf(mw, dpp<ROR4>(mw)); mw = fmaxf(mw, dpp<ROR8>(mw)); mw = max32(max16(mw));
  float lsum = 0.f;
#pragma unroll
  for (int g = 0; g < NR; ++g) { sc[g] = fexp2((sc[g] - mw) * L2E); lsum += sc[g]; }
  float acc[8] = {0.f, 0.f, 0.f, 0.f, 0.f, 0.f, 0.f, 0.f};
  static_for<NR, 2 * NR - WIN>([&](auto G) { constexpr int g = G;
    pv_acc(buf[g % RING], sc[g - NR], acc);
    PIN8(acc);
    SCHED();
    PACE(g + WIN);
    LOAD_GROUP(g + WIN);
    SCHED();
  });
#pragma unroll
  for (int g = 2 * NR - WIN; g < 2 * NR; ++g) pv_acc(buf[g % RING], sc[g - NR], acc);

  {
    float l = lsum; l += dpp<ROR4>(l); l += dpp<ROR8>(l); l = fold16(l, l); l = fold32(l, l);
    L.pr[wave][4 * c + r] = fold8_rows(acc);
    if (lane == 0) L.ml[wave] = f32x2{mw, l};
  }
  __syncthreads();
  if (wave == 0) {
    const f32x2 ml = L.ml[lane & 7];
    f32x2 pa[8];
#pragma unroll
    for (int w = 0; w < 8; ++w) pa[w] = L.pr[w][lane];
    float M = fmaxf(ml.x, dpp<QX1>(ml.x)); M = fmaxf(M, dpp<QX2>(M)); M = fmaxf(M, dpp<HMIR>(M));
    const float f = fexp2((ml.x - M) * L2E);
    float l = f * ml.y; l += dpp<QX1>(l); l += dpp<QX2>(l); l += dpp<HMIR>(l);
    f32x2 o = dpp<NBC + 0>(f) * pa[0];
    o += dpp<NBC + 1>(f) * pa[1]; o += dpp<NBC + 2>(f) * pa[2]; o += dpp<NBC + 3>(f) * pa[3];
    o += dpp<NBC + 4>(f) * pa[4]; o += dpp<NBC + 5>(f) * pa[5]; o += dpp<NBC + 6>(f) * pa[6]; o += dpp<NBC + 7>(f) * pa[7];
    st_unit(rs, WS_AP + (h * 8 + s) * 1024 + lane * 16, asu(o.x), asu(o.y), tag);
    if (lane == 0) st_unit(rs, WS_ML + (h * 8 + s) * 16, asu(M), asu(l), tag);
  }
  __builtin_amdgcn_s_barrier();

  if (roleW) {
    const int sb = lane >> 5, hh = 4 * kblk + qa, dpW = 8 * wave + 4 * ((lane >> 4) & 1) + (lane & 3);
    int off[5]; u32x4 a[5];
    off[0] = WS_ML + (hh * 8 + 2 * (lane & 3) + sb) * 16;
#pragma unroll
    for (int k = 0; k < 4; ++k) off[1 + k] = WS_AP + (hh * 8 + 2 * k + sb) * 1024 + dpW * 16;
    u32 wp01[8], wp23[8]; pair_rows(wW[0], wW[1], wp01); pair_rows(wW[2], wW[3], wp23);
    PIN8(wp01); PIN8(wp23);
    asm volatile("" : "+v"(off[0]), "+v"(off[1]), "+v"(off[2]), "+v"(off[3]), "+v"(off[4]));
    SCHED();
    if (wave == 0) watch_units(rs, WS_ML + (32 * kblk + (lane & 31)) * 16, tag);
    __syncthreads();
#pragma unroll
    for (int k = 0; k < 5; ++k) a[k] = ld_unit(rs, off[k]);
    wait_units<5, 1>(rs, off, a, tag);
    const float m = asf(a[0].x);
    float M = fmaxf(m, dpp<QX1>(m)); M = fmaxf(M, dpp<QX2>(M)); M = max32(M);
    const float w = fexp2((m - M) * L2E);
    float l = w * asf(a[0].z); l += dpp<QX1>(l); l += dpp<QX2>(l); l = fold32(l, l);
    const float w0 = dpp<0x00>(w), w1 = dpp<0x55>(w), w2 = dpp<0xAA>(w), w3 = dpp<0xFF>(w);
    const float o0 = fmaf(w3, asf(a[4].x), fmaf(w2, asf(a[3].x), fmaf(w1, asf(a[2].x), w0 * asf(a[1].x))));
    const float o1 = fmaf(w3, asf(a[4].z), fmaf(w2, asf(a[3].z), fmaf(w1, asf(a[2].z), w0 * asf(a[1].z))));
    const float o = fold32(o0, o1) * frcp(l);
    const u32 p01 = cvtpk(dpp<0x00>(o), dpp<0x55>(o)), p23 = cvtpk(dpp<0xAA>(o), dpp<0xFF>(o));
    float y[8];
#pragma unroll
    for (int j = 0; j < 8; ++j) y[j] = dot2(wp23[j], p23, dot2(wp01[j], p01, 0.f));
    f32x2 t = fold8_rows(y);
    t.x += dpp<ROR8>(t.x); t.y += dpp<ROR8>(t.y); t.x += dpp<ROR4>(t.x); t.y += dpp<ROR4>(t.y);
    if (c < 4) L.red[wave][4 * c + r] = t;
    __syncthreads();
    if (wave == 0) {
      f32x2 z = L.red[2 * r][c] + L.red[2 * r + 1][c];
      z.x = fold16(z.x, z.x); z.y = fold16(z.y, z.y); z.x = fold32(z.x, z.x); z.y = fold32(z.y, z.y);
      if (lane < 16) st_unit(rs, WS_YP + (kblk * 320 + 16 * nblk + lane) * 16, asu(z.x), asu(z.y), tag);
    }
  }

  if (roleF) {
    u32x4 wF[10]; u32 xF[3];
    const u16* wm = ((fb & 1) ? p.wu : p.wg) + 64 * (fb >> 1) + 8 * (lane & 7);
#pragma unroll
    for (int j = 0; j < 5; ++j) { const int pp = (lane >> 3) + 8 * wave + 64 * j;
      wF[2 * j] = ld_nt(wm + (size_t)(2 * pp) * F); wF[2 * j + 1] = ld_nt(wm + (size_t)(2 * pp + 1) * F); }
    int off[5], hidx[3];
#pragma unroll
    for (int j = 0; j < 5; ++j) off[j] = WS_YP + ((lane >> 3) * 320 + 40 * wave + 8 * j + (lane & 7)) * 16;
#pragma unroll
    for (int t = 0; t < 3; ++t) { const int pp = 40 * wave + 8 * (t < 2 ? 2 * t + ((lane >> 4) & 1) : 4) + (lane & 7); xF[t] = xw[pp]; hidx[t] = 2 * pp + (lane >> 5); }
    SCHED();
    u32 wp[5][8];
#pragma unroll
    for (int j = 0; j < 5; ++j) { pair_rows(wF[2 * j], wF[2 * j + 1], wp[j]); PIN8(wp[j]); }
    float xr[3];
#pragma unroll
    for (int t = 0; t < 3; ++t) xr[t] = lane < 32 ? lo16(xF[t]) : hi16(xF[t]);
    {
      float cs[8];
#pragma unroll
      for (int k = 0; k < 8; ++k) { cs[k] = dot2(wp[0][k], 0x3f803f80u, 0.f);
#pragma unroll
        for (int j = 1; j < 5; ++j) cs[k] = dot2(wp[j][k], 0x3f803f80u, cs[k]); }
      f32x2 t = fold8_rows(cs);
      t.x += dpp<ROR8>(t.x); t.y += dpp<ROR8>(t.y);
      if (c < 8) L.redS[wave][4 * c + r] = t;
    }
    __syncthreads();
    f32x2 S = {0.f, 0.f};
    if (wave == 0) {
      const int e = lane & 31, w0 = 4 * (lane >> 5);
      S = (L.redS[w0][e] + L.redS[w0 + 1][e]) + (L.redS[w0 + 2][e] + L.redS[w0 + 3][e]);
      S.x = fold32(S.x, S.x); S.y = fold32(S.y, S.y);
    }
    asm volatile("" : "+v"(S.x), "+v"(S.y), "+v"(xr[0]), "+v"(xr[1]), "+v"(xr[2]), "+v"(hidx[0]), "+v"(hidx[1]), "+v"(hidx[2]));
    SCHED();
    watch_units(rs, WS_ML + (32 * wave + (lane & 31)) * 16, tag);
    Units5 u;
    gather_units5(p.ws, off, u, tag);
    float hv[3];
    {
      const float t0 = fold32(asf(u.a0.x), asf(u.a0.z)), t1 = fold32(asf(u.a1.x), asf(u.a1.z)), t2 = fold32(asf(u.a2.x), asf(u.a2.z));
      const float t3 = fold32(asf(u.a3.x), asf(u.a3.z)), t4 = fold32(asf(u.a4.x), asf(u.a4.z));
      float a[3] = {fold16(t0, t1), fold16(t2, t3), fold16(t4, t4)};
      u16* hb = reinterpret_cast<u16*>(L.hp);
      u32 hq[3];
#pragma unroll
      for (int t = 0; t < 3; ++t) { a[t] += dpp<ROR8>(a[t]); hq[t] = cvtpk(xr[t] + rbf(a[t]), 0.f); hv[t] = asf(hq[t] << 16); }
      if (!(lane & 8)) { hb[hidx[0]] = (u16)hq[0]; hb[hidx[1]] = (u16)hq[1]; hb[hidx[2]] = (u16)hq[2]; }
    }
    __syncthreads();
    u32 hp[5];
#pragma unroll
    for (int j = 0; j < 5; ++j) hp[j] = L.hp[(lane >> 3) + 8 * wave + 64 * j];
    {
      const float d0 = hv[0] - xpivot, d1 = hv[1] - xpivot, d2 = hv[2] - xpivot;
      float s1 = fmaf(0.5f, d2, d0 + d1), s2 = fmaf(0.5f * d2, d2, fmaf(d0, d0, d1 * d1));
      s1 += dpp<QX1>(s1); s2 += dpp<QX1>(s2); s1 += dpp<QX2>(s1); s2 += dpp<QX2>(s2); s1 += dpp<HMIR>(s1); s2 += dpp<HMIR>(s2);
      s1 = fold16(s1, s1); s2 = fold16(s2, s2); s1 = fold32(s1, s1); s2 = fold32(s2, s2);
      if (lane == 0) L.st[wave] = f32x2{s1, s2};
    }
    float g[8] = {0.f, 0.f, 0.f, 0.f, 0.f, 0.f, 0.f, 0.f};
#pragma unroll
    for (int j = 0; j < 5; ++j)
#pragma unroll
      for (int k = 0; k < 8; ++k) g[k] = dot2(wp[j][k], hp[j], g[k]);
    f32x2 t = fold8_rows(g);
    t.x += dpp<ROR8>(t.x); t.y += dpp<ROR8>(t.y);
    if (c < 8) L.red[wave][4 * c + r] = t;
    __syncthreads();
    if (wave == 0) {
      const int e = lane & 31, w0 = 4 * (lane >> 5);
      f32x2 sv = L.st[lane & 7];
      f32x2 A = (L.red[w0][e] + L.red[w0 + 1][e]) + (L.red[w0 + 2][e] + L.red[w0 + 3][e]);
      sv.x += dpp<QX1>(sv.x); sv.y += dpp<QX1>(sv.y); sv.x += dpp<QX2>(sv.x); sv.y += dpp<QX2>(sv.y); sv.x += dpp<HMIR>(sv.x); sv.y += dpp<HMIR>(sv.y);
      A.x = fold32(A.x, A.x); A.y = fold32(A.y, A.y);
      const float e1 = sv.x * (1.f / D), e2 = sv.y * (1.f / D);
      const float meanb = rbf(xpivot + e1), rden = frcp(rbf(__builtin_amdgcn_sqrtf(rbf(rbf(fmaxf(e2 - e1 * e1, 0.f)) + EPS))));
      const u32 ap = cvtpk((A.x - meanb * S.x) * rden, (A.y - meanb * S.y) * rden);
      const u32 nx = asu(dpp<QX1>(asf(ap)));
      if (lane < 32 && !(lane & 1)) st_unit(rs, WS_AC + ((fb & 1) * 432 + 16 * (fb >> 1) + (lane >> 1)) * 16, ap, nx, tag);
    }
  }

  if (roleD) {
    u32x4 wD[8];
#pragma unroll
    for (int j = 0; j < 4; ++j) { int pp = (lane >> 1) + 32 * wave + 256 * j; pp = pp < 864 ? pp : 863;
      wD[2 * j] = ld_nt(p.wd + (size_t)(2 * pp) * D + 16 * cb + 8 * (lane & 1)); wD[2 * j + 1] = ld_nt(p.wd + (size_t)(2 * pp + 1) * D + 16 * cb + 8 * (lane & 1)); }
    const u32 xres = xw[8 * cb + (lane & 7)];
    SCHED();
    const bool has3 = wave < 3;
    u32 wp[4][8];
#pragma unroll
    for (int j = 0; j < 4; ++j) pair_rows(wD[2 * j], wD[2 * j + 1], wp[j]);
#pragma unroll
    for (int k = 0; k < 8; ++k) wp[3][k] = has3 ? wp[3][k] : 0u;
    PIN8(wp[0]); PIN8(wp[1]); PIN8(wp[2]); PIN8(wp[3]);
    SCHED();
    u32 hres = 0;
    if (wave == 3) {
      const int off[1] = {WS_YP + ((lane >> 3) * 320 + 8 * cb + (lane & 7)) * 16}; u32x4 y[1] = {ld_unit(rs, off[0])};
      wait_units<1, 4>(rs, off, y, tag);
      float a0 = asf(y[0].x), a1 = asf(y[0].z);
      a0 += dpp<ROR8>(a0); a1 += dpp<ROR8>(a1); a0 = fold16(a0, a0); a1 = fold16(a1, a1); a0 = fold32(a0, a0); a1 = fold32(a1, a1);
      hres = cvtpk(lo16(xres) + rbf(a0), hi16(xres) + rbf(a1));
    }
    if (wave < 4) {
      const int u1 = tid + 256 < 432 ? tid + 256 : 431;
      const int off[4] = {WS_AC + tid * 16, WS_AC + (432 + tid) * 16, WS_AC + u1 * 16, WS_AC + (432 + u1) * 16};
      u32x4 a[4] = {ld_unit(rs, off[0]), ld_unit(rs, off[1]), ld_unit(rs, off[2]), ld_unit(rs, off[3])};
      wait_units<4, 1>(rs, off, a, tag);
      u32 av[4];
#pragma unroll
      for (int k = 0; k < 4; ++k) { const u32x4 ga = a[k & 2], ua = a[(k & 2) + 1]; const u32 gg = (k & 1) ? ga.z : ga.x, uv = (k & 1) ? ua.z : ua.x; const float g0 = lo16(gg), g1 = hi16(gg);
        av[k] = cvtpk(rbf(g0 * frcp(1.f + fexp2(-g0 * L2E))) * lo16(uv), rbf(g1 * frcp(1.f + fexp2(-g1 * L2E))) * hi16(uv)); }
      *reinterpret_cast<u32x2*>(&L.act[2 * tid]) = u32x2{av[0], av[1]};
      if (tid + 256 < 432) *reinterpret_cast<u32x2*>(&L.act[2 * (tid + 256)]) = u32x2{av[2], av[3]};
    }
    __syncthreads();
    float o[8] = {0.f, 0.f, 0.f, 0.f, 0.f, 0.f, 0.f, 0.f};
#pragma unroll
    for (int j = 0; j < 4; ++j) {
      const u32 av = L.act[(lane >> 1) + 32 * wave + ((j < 3 || has3) ? 256 * j : 0)];
#pragma unroll
      for (int k = 0; k < 8; ++k) o[k] = dot2(wp[j][k], av, o[k]);
    }
    f32x2 t = fold8_rows(o);
    t.x += dpp<ROR8>(t.x); t.y += dpp<ROR8>(t.y); t.x += dpp<ROR4>(t.x); t.y += dpp<ROR4>(t.y); t.x += dpp<QX2>(t.x); t.y += dpp<QX2>(t.y);
    if (c < 2) L.red[wave][4 * c + r] = t;
    __syncthreads();
    if (wave == 3) {
      f32x2 z = L.red[lane >> 3][lane & 7];
      z.x += dpp<ROR8>(z.x); z.y += dpp<ROR8>(z.y); z.x = fold16(z.x, z.x); z.y = fold16(z.y, z.y); z.x = fold32(z.x, z.x); z.y = fold32(z.y, z.y);
      const u32 ov = cvtpk(lo16(hres) + rbf(z.x), hi16(hres) + rbf(z.y));
      if (lane < 8) __hip_atomic_store((GAS u32*)p.out + 8 * cb + lane, ov, __ATOMIC_RELAXED, __HIP_MEMORY_SCOPE_AGENT);
      if (cb == 0 && lane == 0) __hip_atomic_store((GAS u32*)(p.ws + WS_EPOCH), tag, __ATOMIC_RELAXED, __HIP_MEMORY_SCOPE_AGENT);
    }
  }
}

extern "C" void kernel_launch(void* const* d_in, const int* in_sizes, int n_in, void* d_out, int out_size, void* d_ws, size_t ws_size,
                              hipStream_t stream) {
  Params p;
  p.x = (const u16*)d_in[0]; p.K = (const u16*)d_in[1]; p.V = (const u16*)d_in[2]; p.wq = (const u16*)d_in[3]; p.wo = (const u16*)d_in[4];
  p.wg = (const u16*)d_in[6]; p.wu = (const u16*)d_in[7]; p.wd = (const u16*)d_in[8];
  p.out = (u16*)d_out; p.ws = (unsigned char*)d_ws;
  fused_block<<<NBLK, NTHR, 0, stream>>>(p);
}
```

```cpp
#include <hip/hip_runtime.h>
#include <hip/hip_bf16.h>
#include <stdint.h>
#include <type_traits>

typedef unsigned u32;
typedef unsigned long long u64;
typedef unsigned short u16;
typedef u32 u32x4 __attribute__((ext_vector_type(4)));
typedef u32 u32x2 __attribute__((ext_vector_type(2)));
typedef float f32x2 __attribute__((ext_vector_type(2)));
typedef __bf16 bf16x2 __attribute__((ext_vector_type(2)));
#define GAS __attribute__((address_space(1)))

constexpr int D = 640, H = 32, SK = 16384, DH = 128, F = 1728, HD = H * DH;
constexpr int NBLK = 256, NTHR = 512;
constexpr int NR = 16;
constexpr int WIN = 2;
constexpr int RING = 3;
constexpr u32 PACE_DT = 1760;
constexpr u32 START_OFF = 60;
constexpr float L2E = 1.4426950408889634f;
constexpr float SCALE = 0.08838834764831845f;
constexpr float EPS = 1e-5f;
constexpr int WS_EPOCH = 0;
constexpr int WS_QP = 4096;
constexpr int WS_AP = WS_QP + 256 * 1024;
constexpr int WS_ML = WS_AP + 256 * 1024;
constexpr int WS_YP = WS_ML + 4096;
constexpr int WS_AC = WS_YP + 8 * 320 * 16;
constexpr int WS_END = WS_AC + 2 * 432 * 16;
constexpr int SPIN_CAP = 1 << 18;

struct Params {
  const u16 *x, *K, *V, *wq, *wo, *wg, *wu, *wd;
  u16* out;
  unsigned char* ws;
};

template <int I, int N, class Fn> __device__ __forceinline__ void static_for(Fn&& f) { if constexpr (I < N) { f(std::integral_constant<int, I>{}); static_for<I + 1, N>(f); } }
#define SCHED() __builtin_amdgcn_sched_barrier(0)
#define MEMFENCE() asm volatile("" ::: "memory")
#define PIN8(a) asm volatile("" : "+v"(a[0]), "+v"(a[1]), "+v"(a[2]), "+v"(a[3]), "+v"(a[4]), "+v"(a[5]), "+v"(a[6]), "+v"(a[7]))
__device__ __forceinline__ float asf(u32 x) { return __builtin_bit_cast(float, x); }
__device__ __forceinline__ u32 asu(float x) { return __builtin_bit_cast(u32, x); }
__device__ __forceinline__ float dot2(u32 a, u32 b, float c) {
  return __builtin_amdgcn_fdot2_f32_bf16(__builtin_bit_cast(bf16x2, a), __builtin_bit_cast(bf16x2, b), c, false);
}
__device__ __forceinline__ u32 cvtpk(float lo, float hi) { bf16x2 v = {(__bf16)lo, (__bf16)hi}; return __builtin_bit_cast(u32, v); }
__device__ __forceinline__ float rbf(float x) { return asf(cvtpk(x, x) << 16); }
__device__ __forceinline__ float lo16(u32 v) { return asf(v << 16); }
__device__ __forceinline__ float hi16(u32 v) { return asf(v & 0xffff0000u); }
__device__ __forceinline__ float frcp(float x) { return __builtin_amdgcn_rcpf(x); }
__device__ __forceinline__ float fexp2(float x) { return __builtin_amdgcn_exp2f(x); }
template <int CTRL> __device__ __forceinline__ float dpp(float x) {
  return asf((u32)__builtin_amdgcn_mov_dpp((int)asu(x), CTRL, 0xf, 0xf, true));
}
constexpr int QX1 = 0xB1, QX2 = 0x4E, HMIR = 0x141, ROR4 = 0x124, ROR8 = 0x128, NBC = 0x150;
__device__ __forceinline__ float fold32(float a, float b) {
  auto r = __builtin_amdgcn_permlane32_swap(asu(a), asu(b), false, false); return asf(r[0]) + asf(r[1]); }
__device__ __forceinline__ float fold16(float a, float b) {
  auto r = __builtin_amdgcn_permlane16_swap(asu(a), asu(b), false, false); return asf(r[0]) + asf(r[1]); }
__device__ __forceinline__ float max32(float a) {
  auto r = __builtin_amdgcn_permlane32_swap(asu(a), asu(a), false, false); return fmaxf(asf(r[0]), asf(r[1])); }
__device__ __forceinline__ float max16(float a) {
  auto r = __builtin_amdgcn_permlane16_swap(asu(a), asu(a), false, false); return fmaxf(asf(r[0]), asf(r[1])); }
__device__ __forceinline__ float wave_sum(float v) {
  v += dpp<QX1>(v); v += dpp<QX2>(v); v += dpp<HMIR>(v); v += dpp<ROR8>(v);
  v = fold16(v, v); return fold32(v, v);
}
__device__ __forceinline__ f32x2 fold8_rows(const float (&a)[8]) {
  float t0 = fold32(a[0], a[4]), t1 = fold32(a[1], a[5]), t2 = fold32(a[2], a[6]), t3 = fold32(a[3], a[7]);
  return f32x2{fold16(t0, t2), fold16(t1, t3)};
}
__device__ __forceinline__ void pair_rows(u32x4 a, u32x4 b, u32 (&o)[8]) {
#pragma unroll
  for (int k = 0; k < 4; ++k) { o[2 * k] = __builtin_amdgcn_perm(b[k], a[k], 0x05040100u); o[2 * k + 1] = __builtin_amdgcn_perm(b[k], a[k], 0x07060302u); }
}
__device__ __forceinline__ u32x4 ld_nt(const u16* p) { return __builtin_nontemporal_load(reinterpret_cast<const u32x4*>(p)); }

__device__ __forceinline__ u32x4 ld_unit(__amdgpu_buffer_rsrc_t rs, int off) { return __builtin_amdgcn_raw_buffer_load_b128(rs, off, 0, 16); }
__device__ __forceinline__ void st_unit(__amdgpu_buffer_rsrc_t rs, int off, u32 a, u32 b, u32 tag) {
  __builtin_amdgcn_raw_buffer_store_b128(u32x4{a, tag, b, tag}, rs, off, 0, 16);
}
template <int N, int SLEEP> __device__ __forceinline__ void wait_units(__amdgpu_buffer_rsrc_t rs, const int (&off)[N], u32x4 (&v)[N], u32 tag) {
#pragma nounroll
  for (int spin = SPIN_CAP; spin > 0; --spin) {
    u32 bad = 0u;
#pragma unroll
    for (int j = 0; j < N; ++j) bad |= (v[j].y ^ tag) | (v[j].w ^ tag);
    if (__builtin_amdgcn_ballot_w64(bad != 0u) == 0ull) break;
    __builtin_amdgcn_s_sleep(SLEEP);
    MEMFENCE();
#pragma unroll
    for (int j = 0; j < N; ++j) v[j] = ld_unit(rs, off[j]);
  }
}
template <int TH> __device__ __forceinline__ void watch_units(__amdgpu_buffer_rsrc_t rs, int off, u32 tag) {
#pragma nounroll
  for (int spin = SPIN_CAP; spin > 0; --spin) {
    const u32x4 v = ld_unit(rs, off);
    if (__builtin_popcountll(__builtin_amdgcn_ballot_w64(((v.y ^ tag) | (v.w ^ tag)) != 0u)) <= TH) break;
    __builtin_amdgcn_s_sleep(4);
    MEMFENCE();
  }
}
#define UL(r0, r3, o) "global_load_dwordx4 v[" #r0 ":" #r3 "], %[" #o "], %[base] sc1\n\t"
#define UCHK(r0, r1, r3, o, l) \
    "v_cmp_ne_u32 vcc, %[tag], v" #r1 "\n\tv_cmp_ne_u32 %[tmp], %[tag], v" #r3 "\n\ts_or_b64 vcc, vcc, %[tmp]\n\t" \
    "s_cbranch_scc0 " #l "f\n\t" UL(r0, r3, o) "s_mov_b32 %[any], 1\n\t" #l ":\n\t"
struct Units5 { u32x4 a0, a1, a2, a3, a4; };
__device__ __forceinline__ void gather_units5(const unsigned char* ws, const int (&off)[5], Units5& u, u32 tag) {
  int spin = SPIN_CAP; u32 any; u64 tmp;
  asm volatile(UL(232, 235, o0) UL(236, 239, o1) UL(240, 243, o2) UL(244, 247, o3) UL(248, 251, o4)
               "1:\n\ts_waitcnt vmcnt(0)\n\ts_mov_b32 %[any], 0\n\t"
               UCHK(232, 233, 235, o0, 3) UCHK(236, 237, 239, o1, 4) UCHK(240, 241, 243, o2, 5) UCHK(244, 245, 247, o3, 6) UCHK(248, 249, 251, o4, 7)
               "s_cmp_eq_u32 %[any], 0\n\ts_cbranch_scc1 9f\n\ts_sub_u32 %[spin], %[spin], 1\n\ts_cmp_eq_u32 %[spin], 0\n\ts_cbranch_scc1 8f\n\t"
               "s_sleep 1\n\ts_branch 1b\n\t8:\n\ts_waitcnt vmcnt(0)\n\t9:"
               : "=&{v[232:235]}"(u.a0), "=&{v[236:239]}"(u.a1), "=&{v[240:243]}"(u.a2), "=&{v[244:247]}"(u.a3), "=&{v[248:251]}"(u.a4),
                 [spin] "+s"(spin), [any] "=&s"(any), [tmp] "=&s"(tmp)
               : [o0] "v"(off[0]), [o1] "v"(off[1]), [o2] "v"(off[2]), [o3] "v"(off[3]), [o4] "v"(off[4]), [base] "s"(ws), [tag] "s"(tag)
               : "memory", "vcc", "scc");
}

struct Lds {
  f32x2 pr[8][64];
  u32 q[64];
  f32x2 ml[8];
  f32x2 red[8][32];
  f32x2 redS[8][32];
  u32 hp[320];
  u32 act[864];
  f32x2 st[8];
  float xmean; u32 tag, pad0[2];
};

__device__ __forceinline__ void qk_dots(const u32x4 (&k)[4], const u32 (&q)[4], float (&d)[4]) {
#pragma unroll
  for (int i = 0; i < 4; ++i) { float a = dot2(k[i][0], q[0], 0.f); a = dot2(k[i][1], q[1], a); a = dot2(k[i][2], q[2], a); d[i] = dot2(k[i][3], q[3], a); }
}
__device__ __forceinline__ float tile_score(const float (&d)[4], bool b8, bool b4) {
  const float e0 = (b8 ? d[1] : d[0]) + dpp<ROR8>(b8 ? d[0] : d[1]);
  const float e1 = (b8 ? d[3] : d[2]) + dpp<ROR8>(b8 ? d[2] : d[3]);
  float s = (b4 ? e1 : e0) + dpp<HMIR>(b4 ? e0 : e1);
  s += dpp<QX1>(s); s += dpp<QX2>(s);
  return rbf(rbf(s) * SCALE);
}
__device__ __forceinline__ void pv_acc(const u32x4 (&v)[4], float p, float (&acc)[8]) {
  const float pp[4] = {dpp<NBC + 0>(p), dpp<NBC + 8>(p), dpp<NBC + 4>(p), dpp<NBC + 12>(p)};
#pragma unroll
  for (int i = 0; i < 4; ++i)
#pragma unroll
    for (int k = 0; k < 4; ++k) { acc[2 * k] = fmaf(pp[i], lo16(v[i][k]), acc[2 * k]); acc[2 * k + 1] = fmaf(pp[i], hi16(v[i][k]), acc[2 * k + 1]); }
}

__global__ void __launch_bounds__(NTHR, 2) fused_block(Params p) {
  __shared__ Lds L;
  const int tid = threadIdx.x, lane = tid & 63, wave = __builtin_amdgcn_readfirstlane(tid >> 6);
  const int b = blockIdx.x;
  const int h = b >> 3, s = (b + h) & 7;
  const int r = lane >> 4, c = lane & 15;
  const __amdgpu_buffer_rsrc_t rs = __builtin_amdgcn_make_buffer_rsrc(p.ws, 0, WS_END, 0x00020000);
  const int xb = b & 7, tb = (b >> 3) - 20;
  const bool roleW = tb < 0;
  const int kblk = xb, nblk = b >> 3;
  const bool roleD = tb >= 0 && tb < (xb < 2 ? 8 : 4);
  const int cb = 4 * (xb + 8 * (tb >> 2)) + (tb & 3);
  const int fb = xb < 2 ? 2 * (24 + 2 * xb + ((tb - 8) >> 1)) + (tb & 1) : 2 * (4 * (xb - 2) + ((tb - 4) >> 1)) + (tb & 1);
  const bool roleF = tb >= 0 && !roleD && fb < 54;
  const int qa = (lane >> 2) & 3;

  const size_t kvoff = ((size_t)h * SK + s * 2048 + wave * 16 + r) * DH + 8 * c;
  u32x4 buf[RING][4];
#define LOAD_GROUP(g) do { const u16* gp_ = ((g) < NR ? p.K : p.V) + kvoff + (size_t)((g) % NR) * (128 * DH); \
    _Pragma("unroll") for (int j_ = 0; j_ < 4; ++j_) buf[(g) % RING][j_] = ld_nt(gp_ + j_ * 4 * DH); } while (0)
  u64 tpace0;
#define PACE(g) do { const u32 due_ = ((u32)(g) * PACE_DT + (u32)wave * (PACE_DT / 8)) >> 4; \
    while ((int)((u32)(__builtin_amdgcn_s_memrealtime() - tpace0) - due_) < 0) __builtin_amdgcn_s_sleep(1); } while (0)

  const u32 ep_raw = __hip_atomic_load((GAS u32*)(p.ws + WS_EPOCH), __ATOMIC_RELAXED, __HIP_MEMORY_SCOPE_AGENT);
  const u32* xw = reinterpret_cast<const u32*>(p.x);
  u32x4 wW[4];
  {
    const bool qw = wave < 5;
    u32 xa[5], xp[2]; u32x4 wqv[4];
#pragma unroll
    for (int j = 0; j < 5; ++j) xa[j] = xw[lane + 64 * j];
#pragma unroll
    for (int m = 0; m < 2; ++m) xp[m] = xw[qw ? 40 * s + 4 * (2 * wave + m) + r : 0];
#pragma unroll
    for (int m = 0; m < 2; ++m) { const int pp = 40 * s + 4 * (2 * wave + m) + r;
      wqv[2 * m] = ld_nt(qw ? p.wq + (size_t)(2 * pp) * HD + h * DH + 8 * c : p.wq);
      wqv[2 * m + 1] = ld_nt(qw ? p.wq + (size_t)(2 * pp + 1) * HD + h * DH + 8 * c : p.wq); }
    SCHED();
    asm volatile("" : "+v"(xa[0]), "+v"(xa[4]));
    const float c0 = lo16(__builtin_amdgcn_readfirstlane(xa[0]));
    float s1 = 0.f, s2 = 0.f;
#pragma unroll
    for (int j = 0; j < 5; ++j) { const float a = lo16(xa[j]) - c0, bb = hi16(xa[j]) - c0; s1 += a + bb; s2 = fmaf(a, a, fmaf(bb, bb, s2)); }
    s1 = wave_sum(s1) * (1.f / D); s2 = wave_sum(s2) * (1.f / D);
    const float meanb = rbf(c0 + s1), rden = frcp(rbf(__builtin_amdgcn_sqrtf(rbf(rbf(fmaxf(s2 - s1 * s1, 0.f)) + EPS))));
    float a[8] = {0.f, 0.f, 0.f, 0.f, 0.f, 0.f, 0.f, 0.f};
#pragma unroll
    for (int m = 0; m < 2; ++m) {
      const u32 xn = cvtpk(rbf(lo16(xp[m]) - meanb) * rden, rbf(hi16(xp[m]) - meanb) * rden);
      u32 wp[8]; pair_rows(wqv[2 * m], wqv[2 * m + 1], wp);
#pragma unroll
      for (int j = 0; j < 8; ++j) a[j] = dot2(wp[j], xn, a[j]);
    }
    f32x2 t = fold8_rows(a);
    u32 wmask = roleW ? ~0u : 0u;
    asm volatile("" : "+v"(t.x), "+v"(t.y), "+v"(wmask));
    SCHED();
#pragma unroll
    for (int k = 0; k < 4; ++k)
      wW[k] = ld_nt(p.wo + ((u32)((512 * kblk + 128 * qa + 2 * (8 * wave + 4 * ((lane >> 4) & 1) + k) + (lane >> 5)) * D + 32 * nblk + 8 * (lane & 3)) & wmask));
    SCHED();
    if (qw) L.pr[wave][4 * c + r] = t;
    if (tid == 0) { L.xmean = meanb; L.tag = ep_raw + 1u; }
  }
  __syncthreads();
  tpace0 = __builtin_amdgcn_s_memrealtime() + START_OFF;
  if (wave == 0) {
    const u32 tg = __builtin_amdgcn_readfirstlane(ep_raw) + 1u;
    f32x2 q = L.pr[0][lane];
#pragma unroll
    for (int w = 1; w < 5; ++w) q += L.pr[w][lane];
    st_unit(rs, WS_QP + (h * 8 + s) * 1024 + lane * 16, asu(q.x), asu(q.y), tg);
    int off[8]; u32x4 qu[8];
#pragma unroll
    for (int k = 0; k < 8; ++k) { off[k] = WS_QP + (8 * h + k) * 1024 + lane * 16; qu[k] = ld_unit(rs, off[k]); }
    wait_units<8, 1>(rs, off, qu, tg);
    float q0 = 0.f, q1 = 0.f;
#pragma unroll
    for (int k = 0; k < 8; ++k) { q0 += asf(qu[k].x); q1 += asf(qu[k].z); }
    L.q[lane] = cvtpk(q0, q1);
  }
  SCHED();
  static_for<0, RING>([&](auto G) { constexpr int g = G; PACE(g); LOAD_GROUP(g); SCHED(); });
  __syncthreads();
  const u32 tag = __builtin_amdgcn_readfirstlane(L.tag);
  u32 q[4];
  { const u32x4 t = *reinterpret_cast<const u32x4*>(&L.q[4 * c]); q[0] = t.x; q[1] = t.y; q[2] = t.z; q[3] = t.w; }
  const float xpivot = L.xmean;

  const bool b8 = c & 8, b4 = c & 4;
  float sc[NR];
  static_for<0, NR>([&](auto G) { constexpr int g = G;
    float dd[4]; qk_dots(buf[g % RING], q, dd);
    asm volatile("" : "+v"(dd[0]), "+v"(dd[1]), "+v"(dd[2]), "+v"(dd[3]));
    SCHED();
    if constexpr (g + WIN >= RING) { PACE(g + WIN); LOAD_GROUP(g + WIN); }
    SCHED();
    sc[g] = tile_score(dd, b8, b4);
  });
  float mw = sc[0];
#pragma unroll
  for (int g = 1; g < NR; ++g) mw = fmaxf(mw, sc[g]);
  mw = fmaxf(mw, dpp<ROR4>(mw)); mw = fmaxf(mw, dpp<ROR8>(mw)); mw = max32(max16(mw));
  float lsum = 0.f;
#pragma unroll
  for (int g = 0; g < NR; ++g) { sc[g] = fexp2((sc[g] - mw) * L2E); lsum += sc[g]; }
  float acc[8] = {0.f, 0.f, 0.f, 0.f, 0.f, 0.f, 0.f, 0.f};
  static_for<NR, 2 * NR - WIN>([&](auto G) { constexpr int g = G;
    pv_acc(buf[g % RING], sc[g - NR], acc);
    PIN8(acc);
    SCHED();
    PACE(g + WIN);
    LOAD_GROUP(g + WIN);
    SCHED();
  });
#pragma unroll
  for (int g = 2 * NR - WIN; g < 2 * NR; ++g) pv_acc(buf[g % RING], sc[g - NR], acc);

  {
    float l = lsum; l += dpp<ROR4>(l); l += dpp<ROR8>(l); l = fold16(l, l); l = fold32(l, l);
    L.pr[wave][4 * c + r] = fold8_rows(acc);
    if (lane == 0) L.ml[wave] = f32x2{mw, l};
  }
  __syncthreads();
  if (wave == 0) {
    const f32x2 ml = L.ml[lane & 7];
    f32x2 pa[8];
#pragma unroll
    for (int w = 0; w < 8; ++w) pa[w] = L.pr[w][lane];
    float M = fmaxf(ml.x, dpp<QX1>(ml.x)); M = fmaxf(M, dpp<QX2>(M)); M = fmaxf(M, dpp<HMIR>(M));
    const float f = fexp2((ml.x - M) * L2E);
    float l = f * ml.y; l += dpp<QX1>(l); l += dpp<QX2>(l); l += dpp<HMIR>(l);
    f32x2 o = dpp<NBC + 0>(f) * pa[0];
    o += dpp<NBC + 1>(f) * pa[1]; o += dpp<NBC + 2>(f) * pa[2]; o += dpp<NBC + 3>(f) * pa[3];
    o += dpp<NBC + 4>(f) * pa[4]; o += dpp<NBC + 5>(f) * pa[5]; o += dpp<NBC + 6>(f) * pa[6]; o += dpp<NBC + 7>(f) * pa[7];
    st_unit(rs, WS_AP + (h * 8 + s) * 1024 + lane * 16, asu(o.x), asu(o.y), tag);
    if (lane == 0) st_unit(rs, WS_ML + (h * 8 + s) * 16, asu(M), asu(l), tag);
  }
  __builtin_amdgcn_s_barrier();

  if (roleW) {
    const int sb = lane >> 5, hh = 4 * kblk + qa, dpW = 8 * wave + 4 * ((lane >> 4) & 1) + (lane & 3);
    int off[5]; u32x4 a[5];
    off[0] = WS_ML + (hh * 8 + 2 * (lane & 3) + sb) * 16;
#pragma unroll
    for (int k = 0; k < 4; ++k) off[1 + k] = WS_AP + (hh * 8 + 2 * k + sb) * 1024 + dpW * 16;
    u32 wp01[8], wp23[8]; pair_rows(wW[0], wW[1], wp01); pair_rows(wW[2], wW[3], wp23);
    PIN8(wp01); PIN8(wp23);
    asm volatile("" : "+v"(off[0]), "+v"(off[1]), "+v"(off[2]), "+v"(off[3]), "+v"(off[4]));
    SCHED();
    if (wave == 0) watch_units<16>(rs, WS_ML + (32 * kblk + (lane & 31)) * 16, tag);
    __syncthreads();
#pragma unroll
    for (int k = 0; k < 5; ++k) a[k] = ld_unit(rs, off[k]);
    wait_units<5, 1>(rs, off, a, tag);
    const float m = asf(a[0].x);
    float M = fmaxf(m, dpp<QX1>(m)); M = fmaxf(M, dpp<QX2>(M)); M = max32(M);
    const float w = fexp2((m - M) * L2E);
    float l = w * asf(a[0].z); l += dpp<QX1>(l); l += dpp<QX2>(l); l = fold32(l, l);
    const float w0 = dpp<0x00>(w), w1 = dpp<0x55>(w), w2 = dpp<0xAA>(w), w3 = dpp<0xFF>(w);
    const float o0 = fmaf(w3, asf(a[4].x), fmaf(w2, asf(a[3].x), fmaf(w1, asf(a[2].x), w0 * asf(a[1].x))));
    const float o1 = fmaf(w3, asf(a[4].z), fmaf(w2, asf(a[3].z), fmaf(w1, asf(a[2].z), w0 * asf(a[1].z))));
    const float o = fold32(o0, o1) * frcp(l);
    const u32 p01 = cvtpk(dpp<0x00>(o), dpp<0x55>(o)), p23 = cvtpk(dpp<0xAA>(o), dpp<0xFF>(o));
    float y[8];
#pragma unroll
    for (int j = 0; j < 8; ++j) y[j] = dot2(wp23[j], p23, dot2(wp01[j], p01, 0.f));
    f32x2 t = fold8_rows(y);
    t.x += dpp<ROR8>(t.x); t.y += dpp<ROR8>(t.y); t.x += dpp<ROR4>(t.x); t.y += dpp<ROR4>(t.y);
    if (c < 4) L.red[wave][4 * c + r] = t;
    __syncthreads();
    if (wave == 0) {
      f32x2 z = L.red[2 * r][c] + L.red[2 * r + 1][c];
      z.x = fold16(z.x, z.x); z.y = fold16(z.y, z.y); z.x = fold32(z.x, z.x); z.y = fold32(z.y, z.y);
      if (lane < 16) st_unit(rs, WS_YP + (kblk * 320 + 16 * nblk + lane) * 16, asu(z.x), asu(z.y), tag);
    }
  }

  if (roleF) {
    u32x4 wF[10]; u32 xF[3];
    const u16* wm = ((fb & 1) ? p.wu : p.wg) + 64 * (fb >> 1) + 8 * (lane & 7);
#pragma unroll
    for (int j = 0; j < 5; ++j) { const int pp = (lane >> 3) + 8 * wave + 64 * j;
      wF[2 * j] = ld_nt(wm + (size_t)(2 * pp) * F); wF[2 * j + 1] = ld_nt(wm + (size_t)(2 * pp + 1) * F); }
    int off[5], hidx[3];
#pragma unroll
    for (int j = 0; j < 5; ++j) off[j] = WS_YP + ((lane >> 3) * 320 + 40 * wave + 8 * j + (lane & 7)) * 16;
#pragma unroll
    for (int t = 0; t < 3; ++t) { const int pp = 40 * wave + 8 * (t < 2 ? 2 * t + ((lane >> 4) & 1) : 4) + (lane & 7); xF[t] = xw[pp]; hidx[t] = 2 * pp + (lane >> 5); }
    SCHED();
    u32 wp[5][8];
#pragma unroll
    for (int j = 0; j < 5; ++j) { pair_rows(wF[2 * j], wF[2 * j + 1], wp[j]); PIN8(wp[j]); }
    float xr[3];
#pragma unroll
    for (int t = 0; t < 3; ++t) xr[t] = lane < 32 ? lo16(xF[t]) : hi16(xF[t]);
    {
      float cs[8];
#pragma unroll
      for (int k = 0; k < 8; ++k) { cs[k] = dot2(wp[0][k], 0x3f803f80u, 0.f);
#pragma unroll
        for (int j = 1; j < 5; ++j) cs[k] = dot2(wp[j][k], 0x3f803f80u, cs[k]); }
      f32x2 t = fold8_rows(cs);
      t.x += dpp<ROR8>(t.x); t.y += dpp<ROR8>(t.y);
      if (c < 8) L.redS[wave][4 * c + r] = t;
    }
    __syncthreads();
    f32x2 S = {0.f, 0.f};
    if (wave == 0) {
      const int e = lane & 31, w0 = 4 * (lane >> 5);
      S = (L.redS[w0][e] + L.redS[w0 + 1][e]) + (L.redS[w0 + 2][e] + L.redS[w0 + 3][e]);
      S.x = fold32(S.x, S.x); S.y = fold32(S.y, S.y);
    }
    asm volatile("" : "+v"(S.x), "+v"(S.y), "+v"(xr[0]), "+v"(xr[1]), "+v"(xr[2]), "+v"(hidx[0]), "+v"(hidx[1]), "+v"(hidx[2]));
    SCHED();
    watch_units<0>(rs, WS_ML + (32 * wave + (lane & 31)) * 16, tag);
    Units5 u;
    gather_units5(p.ws, off, u, tag);
    float hv[3];
    {
      const float t0 = fold32(asf(u.a0.x), asf(u.a0.z)), t1 = fold32(asf(u.a1.x), asf(u.a1.z)), t2 = fold32(asf(u.a2.x), asf(u.a2.z));
      const float t3 = fold32(asf(u.a3.x), asf(u.a3.z)), t4 = fold32(asf(u.a4.x), asf(u.a4.z));
      float a[3] = {fold16(t0, t1), fold16(t2, t3), fold16(t4, t4)};
      u16* hb = reinterpret_cast<u16*>(L.hp);
      u32 hq[3];
#pragma unroll
      for (int t = 0; t < 3; ++t) { a[t] += dpp<ROR8>(a[t]); hq[t] = cvtpk(xr[t] + rbf(a[t]), 0.f); hv[t] = asf(hq[t] << 16); }
      if (!(lane & 8)) { hb[hidx[0]] = (u16)hq[0]; hb[hidx[1]] = (u16)hq[1]; hb[hidx[2]] = (u16)hq[2]; }
    }
    __syncthreads();
    u32 hp[5];
#pragma unroll
    for (int j = 0; j < 5; ++j) hp[j] = L.hp[(lane >> 3) + 8 * wave + 64 * j];
    {
      const float d0 = hv[0] - xpivot, d1 = hv[1] - xpivot, d2 = hv[2] - xpivot;
      float s1 = fmaf(0.5f, d2, d0 + d1), s2 = fmaf(0.5f * d2, d2, fmaf(d0, d0, d1 * d1));
      s1 += dpp<QX1>(s1); s2 += dpp<QX1>(s2); s1 += dpp<QX2>(s1); s2 += dpp<QX2>(s2); s1 += dpp<HMIR>(s1); s2 += dpp<HMIR>(s2);
      s1 = fold16(s1, s1); s2 = fold16(s2, s2); s1 = fold32(s1, s1); s2 = fold32(s2, s2);
      if (lane == 0) L.st[wave] = f32x2{s1, s2};
    }
    float g[8] = {0.f, 0.f, 0.f, 0.f, 0.f, 0.f, 0.f, 0.f};
#pragma unroll
    for (int j = 0; j < 5; ++j)
#pragma unroll
      for (int k = 0; k < 8; ++k) g[k] = dot2(wp[j][k], hp[j], g[k]);
    f32x2 t = fold8_rows(g);
    t.x += dpp<ROR8>(t.x); t.y += dpp<ROR8>(t.y);
    if (c < 8) L.red[wave][4 * c + r] = t;
    __syncthreads();
    if (wave == 0) {
      const int e = lane & 31, w0 = 4 * (lane >> 5);
      f32x2 sv = L.st[lane & 7];
      f32x2 A = (L.red[w0][e] + L.red[w0 + 1][e]) + (L.red[w0 + 2][e] + L.red[w0 + 3][e]);
      sv.x += dpp<QX1>(sv.x); sv.y += dpp<QX1>(sv.y); sv.x += dpp<QX2>(sv.x); sv.y += dpp<QX2>(sv.y); sv.x += dpp<HMIR>(sv.x); sv.y += dpp<HMIR>(sv.y);
      A.x = fold32(A.x, A.x); A.y = fold32(A.y, A.y);
      const float e1 = sv.x * (1.f / D), e2 = sv.y * (1.f / D);
      const float meanb = rbf(xpivot + e1), rden = frcp(rbf(__builtin_amdgcn_sqrtf(rbf(rbf(fmaxf(e2 - e1 * e1, 0.f)) + EPS))));
      const u32 ap = cvtpk((A.x - meanb * S.x) * rden, (A.y - meanb * S.y) * rden);
      const u32 nx = asu(dpp<QX1>(asf(ap)));
      if (lane < 32 && !(lane & 1)) st_unit(rs, WS_AC + ((fb & 1) * 432 + 16 * (fb >> 1) + (lane >> 1)) * 16, ap, nx, tag);
    }
  }

  if (roleD) {
    u32x4 wD[8];
#pragma unroll
    for (int j = 0; j < 4; ++j) { int pp = (lane >> 1) + 32 * wave + 256 * j; pp = pp < 864 ? pp : 863;
      wD[2 * j] = ld_nt(p.wd + (size_t)(2 * pp) * D + 16 * cb + 8 * (lane & 1)); wD[2 * j + 1] = ld_nt(p.wd + (size_t)(2 * pp + 1) * D + 16 * cb + 8 * (lane & 1)); }
    const u32 xres = xw[8 * cb + (lane & 7)];
    SCHED();
    const bool has3 = wave < 3;
    u32 wp[4][8];
#pragma unroll
    for (int j = 0; j < 4; ++j) pair_rows(wD[2 * j], wD[2 * j + 1], wp[j]);
#pragma unroll
    for (int k = 0; k < 8; ++k) wp[3][k] = has3 ? wp[3][k] : 0u;
    PIN8(wp[0]); PIN8(wp[1]); PIN8(wp[2]); PIN8(wp[3]);
    SCHED();
    u32 hres = 0;
    if (wave == 3) {
      const int off[1] = {WS_YP + ((lane >> 3) * 320 + 8 * cb + (lane & 7)) * 16}; u32x4 y[1] = {ld_unit(rs, off[0])};
      wait_units<1, 4>(rs, off, y, tag);
      float a0 = asf(y[0].x), a1 = asf(y[0].z);
      a0 += dpp<ROR8>(a0); a1 += dpp<ROR8>(a1); a0 = fold16(a0, a0); a1 = fold16(a1, a1); a0 = fold32(a0, a0); a1 = fold32(a1, a1);
      hres = cvtpk(lo16(xres) + rbf(a0), hi16(xres) + rbf(a1));
    }
    if (wave < 4) {
      const int u1 = tid + 256 < 432 ? tid + 256 : 431;
      const int off[4] = {WS_AC + tid * 16, WS_AC + (432 + tid) * 16, WS_AC + u1 * 16, WS_AC + (432 + u1) * 16};
      u32x4 a[4] = {ld_unit(rs, off[0]), ld_unit(rs, off[1]), ld_unit(rs, off[2]), ld_unit(rs, off[3])};
      wait_units<4, 1>(rs, off, a, tag);
      u32 av[4];
#pragma unroll
      for (int k = 0; k < 4; ++k) { const u32x4 ga = a[k & 2], ua = a[(k & 2) + 1]; const u32 gg = (k & 1) ? ga.z : ga.x, uv = (k & 1) ? ua.z : ua.x; const float g0 = lo16(gg), g1 = hi16(gg);
        av[k] = cvtpk(rbf(g0 * frcp(1.f + fexp2(-g0 * L2E))) * lo16(uv), rbf(g1 * frcp(1.f + fexp2(-g1 * L2E))) * hi16(uv)); }
      *reinterpret_cast<u32x2*>(&L.act[2 * tid]) = u32x2{av[0], av[1]};
      if (tid + 256 < 432) *reinterpret_cast<u32x2*>(&L.act[2 * (tid + 256)]) = u32x2{av[2], av[3]};
    }
    __syncthreads();
    float o[8] = {0.f, 0.f, 0.f, 0.f, 0.f, 0.f, 0.f, 0.f};
#pragma unroll
    for (int j = 0; j < 4; ++j) {
      const u32 av = L.act[(lane >> 1) + 32 * wave + ((j < 3 || has3) ? 256 * j : 0)];
#pragma unroll
      for (int k = 0; k < 8; ++k) o[k] = dot2(wp[j][k], av, o[k]);
    }
    f32x2 t = fold8_rows(o);
    t.x += dpp<ROR8>(t.x); t.y += dpp<ROR8>(t.y); t.x += dpp<ROR4>(t.x); t.y += dpp<ROR4>(t.y); t.x += dpp<QX2>(t.x); t.y += dpp<QX2>(t.y);
    if (c < 2) L.red[wave][4 * c + r] = t;
    __syncthreads();
    if (wave == 3) {
      f32x2 z = L.red[lane >> 3][lane & 7];
      z.x += dpp<ROR8>(z.x); z.y += dpp<ROR8>(z.y); z.x = fold16(z.x, z.x); z.y = fold16(z.y, z.y); z.x = fold32(z.x, z.x); z.y = fold32(z.y, z.y);
      const u32 ov = cvtpk(lo16(hres) + rbf(z.x), hi16(hres) + rbf(z.y));
      if (lane < 8) __hip_atomic_store((GAS u32*)p.out + 8 * cb + lane, ov, __ATOMIC_RELAXED, __HIP_MEMORY_SCOPE_AGENT);
      if (cb == 0 && lane == 0) __hip_atomic_store((GAS u32*)(p.ws + WS_EPOCH), tag, __ATOMIC_RELAXED, __HIP_MEMORY_SCOPE_AGENT);
    }
  }
}

extern "C" void kernel_launch(void* const* d_in, const int* in_sizes, int n_in, void* d_out, int out_size, void* d_ws, size_t ws_size,
                              hipStream_t stream) {
  Params p;
  p.x = (const u16*)d_in[0]; p.K = (const u16*)d_in[1]; p.V = (const u16*)d_in[2]; p.wq = (const u16*)d_in[3]; p.wo = (const u16*)d_in[4];
  p.wg = (const u16*)d_in[6]; p.wu = (const u16*)d_in[7]; p.wd = (const u16*)d_in[8];
  p.out = (u16*)d_out; p.ws = (unsigned char*)d_ws;
  fused_block<<<NBLK, NTHR, 0, stream>>>(p);
}
```

```cpp
#include <hip/hip_runtime.h>
#include <hip/hip_bf16.h>
#include <stdint.h>
#include <type_traits>

typedef unsigned u32;
typedef unsigned long long u64;
typedef unsigned short u16;
typedef u32 u32x4 __attribute__((ext_vector_type(4)));
typedef u32 u32x2 __attribute__((ext_vector_type(2)));
typedef float f32x2 __attribute__((ext_vector_type(2)));
typedef __bf16 bf16x2 __attribute__((ext_vector_type(2)));
#define GAS __attribute__((address_space(1)))

constexpr int D = 640, H = 32, SK = 16384, DH = 128, F = 1728, HD = H * DH;
constexpr int NBLK = 256, NTHR = 512;
constexpr int NR = 16;
constexpr int WIN = 2;
constexpr int RING = 3;
constexpr u32 PACE_DT = 1760;
constexpr u32 START_OFF = 60;
constexpr float L2E = 1.4426950408889634f;
constexpr float SCALE = 0.08838834764831845f;
constexpr float EPS = 1e-5f;
constexpr int WS_EPOCH = 0;
constexpr int WS_QP = 4096;
constexpr int WS_AP = WS_QP + 256 * 1024;
constexpr int WS_ML = WS_AP + 256 * 1024;
constexpr int WS_YP = WS_ML + 4096;
constexpr int WS_AC = WS_YP + 8 * 320 * 16;
constexpr int WS_END = WS_AC + 2 * 432 * 16;
constexpr int SPIN_CAP = 1 << 18;

struct Params {
  const u16 *x, *K, *V, *wq, *wo, *wg, *wu, *wd;
  u16* out;
  unsigned char* ws;
};

template <int I, int N, class Fn> __device__ __forceinline__ void static_for(Fn&& f) { if constexpr (I < N) { f(std::integral_constant<int, I>{}); static_for<I + 1, N>(f); } }
#define SCHED() __builtin_amdgcn_sched_barrier(0)
#define MEMFENCE() asm volatile("" ::: "memory")
#define PIN8(a) asm volatile("" : "+v"(a[0]), "+v"(a[1]), "+v"(a[2]), "+v"(a[3]), "+v"(a[4]), "+v"(a[5]), "+v"(a[6]), "+v"(a[7]))
__device__ __forceinline__ float asf(u32 x) { return __builtin_bit_cast(float, x); }
__device__ __forceinline__ u32 asu(float x) { return __builtin_bit_cast(u32, x); }
__device__ __forceinline__ float dot2(u32 a, u32 b, float c) {
  return __builtin_amdgcn_fdot2_f32_bf16(__builtin_bit_cast(bf16x2, a), __builtin_bit_cast(bf16x2, b), c, false);
}
__device__ __forceinline__ u32 cvtpk(float lo, float hi) { bf16x2 v = {(__bf16)lo, (__bf16)hi}; return __builtin_bit_cast(u32, v); }
__device__ __forceinline__ float rbf(float x) { return asf(cvtpk(x, x) << 16); }
__device__ __forceinline__ float lo16(u32 v) { return asf(v << 16); }
__device__ __forceinline__ float hi16(u32 v) { return asf(v & 0xffff0000u); }
__device__ __forceinline__ float frcp(float x) { return __builtin_amdgcn_rcpf(x); }
__device__ __forceinline__ float fexp2(float x) { return __builtin_amdgcn_exp2f(x); }
template <int CTRL> __device__ __forceinline__ float dpp(float x) {
  return asf((u32)__builtin_amdgcn_mov_dpp((int)asu(x), CTRL, 0xf, 0xf, true));
}
constexpr int QX1 = 0xB1, QX2 = 0x4E, HMIR = 0x141, ROR4 = 0x124, ROR8 = 0x128, NBC = 0x150;
__device__ __forceinline__ float fold32(float a, float b) {
  auto r = __builtin_amdgcn_permlane32_swap(asu(a), asu(b), false, false); return asf(r[0]) + asf(r[1]); }
__device__ __forceinline__ float fold16(float a, float b) {
  auto r = __builtin_amdgcn_permlane16_swap(asu(a), asu(b), false, false); return asf(r[0]) + asf(r[1]); }
__device__ __forceinline__ float max32(float a) {
  auto r = __builtin_amdgcn_permlane32_swap(asu(a), asu(a), false, false); return fmaxf(asf(r[0]), asf(r[1])); }
__device__ __forceinline__ float max16(float a) {
  auto r = __builtin_amdgcn_permlane16_swap(asu(a), asu(a), false, false); return fmaxf(asf(r[0]), asf(r[1])); }
__device__ __forceinline__ float wave_sum(float v) {
  v += dpp<QX1>(v); v += dpp<QX2>(v); v += dpp<HMIR>(v); v += dpp<ROR8>(v);
  v = fold16(v, v); return fold32(v, v);
}
__device__ __forceinline__ f32x2 fold8_rows(const float (&a)[8]) {
  float t0 = fold32(a[0], a[4]), t1 = fold32(a[1], a[5]), t2 = fold32(a[2], a[6]), t3 = fold32(a[3], a[7]);
  return f32x2{fold16(t0, t2), fold16(t1, t3)};
}
__device__ __forceinline__ void pair_rows(u32x4 a, u32x4 b, u32 (&o)[8]) {
#pragma unroll
  for (int k = 0; k < 4; ++k) { o[2 * k] = __builtin_amdgcn_perm(b[k], a[k], 0x05040100u); o[2 * k + 1] = __builtin_amdgcn_perm(b[k], a[k], 0x07060302u); }
}
__device__ __forceinline__ u32x4 ld_nt(const u16* p) { return __builtin_nontemporal_load(reinterpret_cast<const u32x4*>(p)); }
__device__ __forceinline__ u32x4 ld_w(const u16* p) { return *reinterpret_cast<const u32x4*>(p); }

__device__ __forceinline__ u32x4 ld_unit(__amdgpu_buffer_rsrc_t rs, int off) { return __builtin_amdgcn_raw_buffer_load_b128(rs, off, 0, 16); }
__device__ __forceinline__ void st_unit(__amdgpu_buffer_rsrc_t rs, int off, u32 a, u32 b, u32 tag) {
  __builtin_amdgcn_raw_buffer_store_b128(u32x4{a, tag, b, tag}, rs, off, 0, 16);
}
template <int N, int SLEEP> __device__ __forceinline__ void wait_units(__amdgpu_buffer_rsrc_t rs, const int (&off)[N], u32x4 (&v)[N], u32 tag) {
#pragma nounroll
  for (int spin = SPIN_CAP; spin > 0; --spin) {
    u32 bad = 0u;
#pragma unroll
    for (int j = 0; j < N; ++j) bad |= (v[j].y ^ tag) | (v[j].w ^ tag);
    if (__builtin_amdgcn_ballot_w64(bad != 0u) == 0ull) break;
    __builtin_amdgcn_s_sleep(SLEEP);
    MEMFENCE();
#pragma unroll
    for (int j = 0; j < N; ++j) v[j] = ld_unit(rs, off[j]);
  }
}
template <int TH> __device__ __forceinline__ void watch_units(__amdgpu_buffer_rsrc_t rs, int off, u32 tag) {
#pragma nounroll
  for (int spin = SPIN_CAP; spin > 0; --spin) {
    const u32x4 v = ld_unit(rs, off);
    if (__builtin_popcountll(__builtin_amdgcn_ballot_w64(((v.y ^ tag) | (v.w ^ tag)) != 0u)) <= TH) break;
    __builtin_amdgcn_s_sleep(4);
    MEMFENCE();
  }
}
#define UL(r0, r3, o) "global_load_dwordx4 v[" #r0 ":" #r3 "], %[" #o "], %[base] sc1\n\t"
#define UCHK(r0, r1, r3, o, l) \
    "v_cmp_ne_u32 vcc, %[tag], v" #r1 "\n\tv_cmp_ne_u32 %[tmp], %[tag], v" #r3 "\n\ts_or_b64 vcc, vcc, %[tmp]\n\t" \
    "s_cbranch_scc0 " #l "f\n\t" UL(r0, r3, o) "s_mov_b32 %[any], 1\n\t" #l ":\n\t"
struct Units5 { u32x4 a0, a1, a2, a3, a4; };
__device__ __forceinline__ void gather_units5(const unsigned char* ws, const int (&off)[5], Units5& u, u32 tag) {
  int spin = SPIN_CAP; u32 any; u64 tmp;
  asm volatile(UL(232, 235, o0) UL(236, 239, o1) UL(240, 243, o2) UL(244, 247, o3) UL(248, 251, o4)
               "1:\n\ts_waitcnt vmcnt(0)\n\ts_mov_b32 %[any], 0\n\t"
               UCHK(232, 233, 235, o0, 3) UCHK(236, 237, 239, o1, 4) UCHK(240, 241, 243, o2, 5) UCHK(244, 245, 247, o3, 6) UCHK(248, 249, 251, o4, 7)
               "s_cmp_eq_u32 %[any], 0\n\ts_cbranch_scc1 9f\n\ts_sub_u32 %[spin], %[spin], 1\n\ts_cmp_eq_u32 %[spin], 0\n\ts_cbranch_scc1 8f\n\t"
               "s_sleep 1\n\ts_branch 1b\n\t8:\n\ts_waitcnt vmcnt(0)\n\t9:"
               : "=&{v[232:235]}"(u.a0), "=&{v[236:239]}"(u.a1), "=&{v[240:243]}"(u.a2), "=&{v[244:247]}"(u.a3), "=&{v[248:251]}"(u.a4),
                 [spin] "+s"(spin), [any] "=&s"(any), [tmp] "=&s"(tmp)
               : [o0] "v"(off[0]), [o1] "v"(off[1]), [o2] "v"(off[2]), [o3] "v"(off[3]), [o4] "v"(off[4]), [base] "s"(ws), [tag] "s"(tag)
               : "memory", "vcc", "scc");
}

struct Lds {
  f32x2 pr[8][64];
  u32 q[64];
  f32x2 ml[8];
  f32x2 red[8][32];
  f32x2 redS[8][32];
  u32 hp[320];
  u32 act[864];
  f32x2 st[8];
  float xmean; u32 tag, pad0[2];
};

__device__ __forceinline__ void qk_dots(const u32x4 (&k)[4], const u32 (&q)[4], float (&d)[4]) {
#pragma unroll
  for (int i = 0; i < 4; ++i) { float a = dot2(k[i][0], q[0], 0.f); a = dot2(k[i][1], q[1], a); a = dot2(k[i][2], q[2], a); d[i] = dot2(k[i][3], q[3], a); }
}
__device__ __forceinline__ float tile_score(const float (&d)[4], bool b8, bool b4) {
  const float e0 = (b8 ? d[1] : d[0]) + dpp<ROR8>(b8 ? d[0] : d[1]);
  const float e1 = (b8 ? d[3] : d[2]) + dpp<ROR8>(b8 ? d[2] : d[3]);
  float s = (b4 ? e1 : e0) + dpp<HMIR>(b4 ? e0 : e1);
  s += dpp<QX1>(s); s += dpp<QX2>(s);
  return rbf(rbf(s) * SCALE);
}
__device__ __forceinline__ void pv_acc(const u32x4 (&v)[4], float p, float (&acc)[8]) {
  const float pp[4] = {dpp<NBC + 0>(p), dpp<NBC + 8>(p), dpp<NBC + 4>(p), dpp<NBC + 12>(p)};
#pragma unroll
  for (int i = 0; i < 4; ++i)
#pragma unroll
    for (int k = 0; k < 4; ++k) { acc[2 * k] = fmaf(pp[i], lo16(v[i][k]), acc[2 * k]); acc[2 * k + 1] = fmaf(pp[i], hi16(v[i][k]), acc[2 * k + 1]); }
}

__global__ void __launch_bounds__(NTHR, 2) fused_block(Params p) {
  __shared__ Lds L;
  const int tid = threadIdx.x, lane = tid & 63, wave = __builtin_amdgcn_readfirstlane(tid >> 6);
  const int b = blockIdx.x;
  const int h = b >> 3, s = (b + h) & 7;
  const int r = lane >> 4, c = lane & 15;
  const __amdgpu_buffer_rsrc_t rs = __builtin_amdgcn_make_buffer_rsrc(p.ws, 0, WS_END, 0x00020000);
  const int xb = b & 7, tb = (b >> 3) - 20;
  const bool roleW = tb < 0;
  const int kblk = xb, nblk = b >> 3;
  const bool roleD = tb >= 0 && tb < (xb < 2 ? 8 : 4);
  const int cb = 4 * (xb + 8 * (tb >> 2)) + (tb & 3);
  const int fb = xb < 2 ? 2 * (24 + 2 * xb + ((tb - 8) >> 1)) + (tb & 1) : 2 * (4 * (xb - 2) + ((tb - 4) >> 1)) + (tb & 1);
  const bool roleF = tb >= 0 && !roleD && fb < 54;
  const int qa = (lane >> 2) & 3;

  const size_t kvoff = ((size_t)h * SK + s * 2048 + wave * 16 + r) * DH + 8 * c;
  u32x4 buf[RING][4];
#define LOAD_GROUP(g) do { const u16* gp_ = ((g) < NR ? p.K : p.V) + kvoff + (size_t)((g) % NR) * (128 * DH); \
    _Pragma("unroll") for (int j_ = 0; j_ < 4; ++j_) buf[(g) % RING][j_] = ld_nt(gp_ + j_ * 4 * DH); } while (0)
  u64 tpace0;
#define PACE(g) do { const u32 due_ = ((u32)(g) * PACE_DT + (u32)wave * (PACE_DT / 8)) >> 4; \
    while ((int)((u32)(__builtin_amdgcn_s_memrealtime() - tpace0) - due_) < 0) __builtin_amdgcn_s_sleep(1); } while (0)

  const u32 ep_raw = __hip_atomic_load((GAS u32*)(p.ws + WS_EPOCH), __ATOMIC_RELAXED, __HIP_MEMORY_SCOPE_AGENT);
  const u32* xw = reinterpret_cast<const u32*>(p.x);
  u32x4 wW[4];
  {
    const bool qw = wave < 5;
    u32 xa[5], xp[2]; u32x4 wqv[4];
#pragma unroll
    for (int j = 0; j < 5; ++j) xa[j] = xw[lane + 64 * j];
#pragma unroll
    for (int m = 0; m < 2; ++m) xp[m] = xw[qw ? 40 * s + 4 * (2 * wave + m) + r : 0];
#pragma unroll
    for (int m = 0; m < 2; ++m) { const int pp = 40 * s + 4 * (2 * wave + m) + r;
      wqv[2 * m] = ld_w(qw ? p.wq + (size_t)(2 * pp) * HD + h * DH + 8 * c : p.wq);
      wqv[2 * m + 1] = ld_w(qw ? p.wq + (size_t)(2 * pp + 1) * HD + h * DH + 8 * c : p.wq); }
    SCHED();
    asm volatile("" : "+v"(xa[0]), "+v"(xa[4]));
    const float c0 = lo16(__builtin_amdgcn_readfirstlane(xa[0]));
    float s1 = 0.f, s2 = 0.f;
#pragma unroll
    for (int j = 0; j < 5; ++j) { const float a = lo16(xa[j]) - c0, bb = hi16(xa[j]) - c0; s1 += a + bb; s2 = fmaf(a, a, fmaf(bb, bb, s2)); }
    s1 = wave_sum(s1) * (1.f / D); s2 = wave_sum(s2) * (1.f / D);
    const float meanb = rbf(c0 + s1), rden = frcp(rbf(__builtin_amdgcn_sqrtf(rbf(rbf(fmaxf(s2 - s1 * s1, 0.f)) + EPS))));
    float a[8] = {0.f, 0.f, 0.f, 0.f, 0.f, 0.f, 0.f, 0.f};
#pragma unroll
    for (int m = 0; m < 2; ++m) {
      const u32 xn = cvtpk(rbf(lo16(xp[m]) - meanb) * rden, rbf(hi16(xp[m]) - meanb) * rden);
      u32 wp[8]; pair_rows(wqv[2 * m], wqv[2 * m + 1], wp);
#pragma unroll
      for (int j = 0; j < 8; ++j) a[j] = dot2(wp[j], xn, a[j]);
    }
    f32x2 t = fold8_rows(a);
    u32 wmask = roleW ? ~0u : 0u;
    asm volatile("" : "+v"(t.x), "+v"(t.y), "+v"(wmask));
    SCHED();
#pragma unroll
    for (int k = 0; k < 4; ++k)
      wW[k] = ld_w(p.wo + ((u32)((512 * kblk + 128 * qa + 2 * (8 * wave + 4 * ((lane >> 4) & 1) + k) + (lane >> 5)) * D + 32 * nblk + 8 * (lane & 3)) & wmask));
    SCHED();
    if (qw) L.pr[wave][4 * c + r] = t;
    if (tid == 0) { L.xmean = meanb; L.tag = ep_raw + 1u; }
  }
  __syncthreads();
  tpace0 = __builtin_amdgcn_s_memrealtime() + START_OFF;
  if (wave == 0) {
    const u32 tg = __builtin_amdgcn_readfirstlane(ep_raw) + 1u;
    f32x2 q = L.pr[0][lane];
#pragma unroll
    for (int w = 1; w < 5; ++w) q += L.pr[w][lane];
    st_unit(rs, WS_QP + (h * 8 + s) * 1024 + lane * 16, asu(q.x), asu(q.y), tg);
    int off[8]; u32x4 qu[8];
#pragma unroll
    for (int k = 0; k < 8; ++k) { off[k] = WS_QP + (8 * h + k) * 1024 + lane * 16; qu[k] = ld_unit(rs, off[k]); }
    wait_units<8, 1>(rs, off, qu, tg);
    float q0 = 0.f, q1 = 0.f;
#pragma unroll
    for (int k = 0; k < 8; ++k) { q0 += asf(qu[k].x); q1 += asf(qu[k].z); }
    L.q[lane] = cvtpk(q0, q1);
  }
  SCHED();
  static_for<0, RING>([&](auto G) { constexpr int g = G; PACE(g); LOAD_GROUP(g); SCHED(); });
  __syncthreads();
  const u32 tag = __builtin_amdgcn_readfirstlane(L.tag);
  u32 q[4];
  { const u32x4 t = *reinterpret_cast<const u32x4*>(&L.q[4 * c]); q[0] = t.x; q[1] = t.y; q[2] = t.z; q[3] = t.w; }
  const float xpivot = L.xmean;

  const bool b8 = c & 8, b4 = c & 4;
  float sc[NR];
  static_for<0, NR>([&](auto G) { constexpr int g = G;
    float dd[4]; qk_dots(buf[g % RING], q, dd);
    asm volatile("" : "+v"(dd[0]), "+v"(dd[1]), "+v"(dd[2]), "+v"(dd[3]));
    SCHED();
    if constexpr (g + WIN >= RING) { PACE(g + WIN); LOAD_GROUP(g + WIN); }
    SCHED();
    sc[g] = tile_score(dd, b8, b4);
  });
  float mw = sc[0];
#pragma unroll
  for (int g = 1; g < NR; ++g) mw = fmaxf(mw, sc[g]);
  mw = fmaxf(mw, dpp<ROR4>(mw)); mw = fmaxf(mw, dpp<ROR8>(mw)); mw = max32(max16(mw));
  float lsum = 0.f;
#pragma unroll
  for (int g = 0; g < NR; ++g) { sc[g] = fexp2((sc[g] - mw) * L2E); lsum += sc[g]; }
  float acc[8] = {0.f, 0.f, 0.f, 0.f, 0.f, 0.f, 0.f, 0.f};
  static_for<NR, 2 * NR - WIN>([&](auto G) { constexpr int g = G;
    pv_acc(buf[g % RING], sc[g - NR], acc);
    PIN8(acc);
    SCHED();
    PACE(g + WIN);
    LOAD_GROUP(g + WIN);
    SCHED();
  });
#pragma unroll
  for (int g = 2 * NR - WIN; g < 2 * NR; ++g) pv_acc(buf[g % RING], sc[g - NR], acc);

  {
    float l = lsum; l += dpp<ROR4>(l); l += dpp<ROR8>(l); l = fold16(l, l); l = fold32(l, l);
    L.pr[wave][4 * c + r] = fold8_rows(acc);
    if (lane == 0) L.ml[wave] = f32x2{mw, l};
  }
  __syncthreads();
  if (wave == 0) {
    const f32x2 ml = L.ml[lane & 7];
    f32x2 pa[8];
#pragma unroll
    for (int w = 0; w < 8; ++w) pa[w] = L.pr[w][lane];
    float M = fmaxf(ml.x, dpp<QX1>(ml.x)); M = fmaxf(M, dpp<QX2>(M)); M = fmaxf(M, dpp<HMIR>(M));
    const float f = fexp2((ml.x - M) * L2E);
    float l = f * ml.y; l += dpp<QX1>(l); l += dpp<QX2>(l); l += dpp<HMIR>(l);
    f32x2 o = dpp<NBC + 0>(f) * pa[0];
    o += dpp<NBC + 1>(f) * pa[1]; o += dpp<NBC + 2>(f) * pa[2]; o += dpp<NBC + 3>(f) * pa[3];
    o += dpp<NBC + 4>(f) * pa[4]; o += dpp<NBC + 5>(f) * pa[5]; o += dpp<NBC + 6>(f) * pa[6]; o += dpp<NBC + 7>(f) * pa[7];
    st_unit(rs, WS_AP + (h * 8 + s) * 1024 + lane * 16, asu(o.x), asu(o.y), tag);
    if (lane == 0) st_unit(rs, WS_ML + (h * 8 + s) * 16, asu(M), asu(l), tag);
  }
  __builtin_amdgcn_s_barrier();

  if (roleW) {
    const int sb = lane >> 5, hh = 4 * kblk + qa, dpW = 8 * wave + 4 * ((lane >> 4) & 1) + (lane & 3);
    int off[5]; u32x4 a[5];
    off[0] = WS_ML + (hh * 8 + 2 * (lane & 3) + sb) * 16;
#pragma unroll
    for (int k = 0; k < 4; ++k) off[1 + k] = WS_AP + (hh * 8 + 2 * k + sb) * 1024 + dpW * 16;
    u32 wp01[8], wp23[8]; pair_rows(wW[0], wW[1], wp01); pair_rows(wW[2], wW[3], wp23);
    PIN8(wp01); PIN8(wp23);
    asm volatile("" : "+v"(off[0]), "+v"(off[1]), "+v"(off[2]), "+v"(off[3]), "+v"(off[4]));
    SCHED();
    if (wave == 0) watch_units<16>(rs, WS_ML + (32 * kblk + (lane & 31)) * 16, tag);
    __syncthreads();
#pragma unroll
    for (int k = 0; k < 5; ++k) a[k] = ld_unit(rs, off[k]);
    wait_units<5, 1>(rs, off, a, tag);
    const float m = asf(a[0].x);
    float M = fmaxf(m, dpp<QX1>(m)); M = fmaxf(M, dpp<QX2>(M)); M = max32(M);
    const float w = fexp2((m - M) * L2E);
    float l = w * asf(a[0].z); l += dpp<QX1>(l); l += dpp<QX2>(l); l = fold32(l, l);
    const float w0 = dpp<0x00>(w), w1 = dpp<0x55>(w), w2 = dpp<0xAA>(w), w3 = dpp<0xFF>(w);
    const float o0 = fmaf(w3, asf(a[4].x), fmaf(w2, asf(a[3].x), fmaf(w1, asf(a[2].x), w0 * asf(a[1].x))));
    const float o1 = fmaf(w3, asf(a[4].z), fmaf(w2, asf(a[3].z), fmaf(w1, asf(a[2].z), w0 * asf(a[1].z))));
    const float o = fold32(o0, o1) * frcp(l);
    const u32 p01 = cvtpk(dpp<0x00>(o), dpp<0x55>(o)), p23 = cvtpk(dpp<0xAA>(o), dpp<0xFF>(o));
    float y[8];
#pragma unroll
    for (int j = 0; j < 8; ++j) y[j] = dot2(wp23[j], p23, dot2(wp01[j], p01, 0.f));
    f32x2 t = fold8_rows(y);
    t.x += dpp<ROR8>(t.x); t.y += dpp<ROR8>(t.y); t.x += dpp<ROR4>(t.x); t.y += dpp<ROR4>(t.y);
    if (c < 4) L.red[wave][4 * c + r] = t;
    __syncthreads();
    if (wave == 0) {
      f32x2 z = L.red[2 * r][c] + L.red[2 * r + 1][c];
      z.x = fold16(z.x, z.x); z.y = fold16(z.y, z.y); z.x = fold32(z.x, z.x); z.y = fold32(z.y, z.y);
      if (lane < 16) st_unit(rs, WS_YP + (kblk * 320 + 16 * nblk + lane) * 16, asu(z.x), asu(z.y), tag);
    }
  }

  if (roleF) {
    u32x4 wF[10]; u32 xF[3];
    const u16* wm = ((fb & 1) ? p.wu : p.wg) + 64 * (fb >> 1) + 8 * (lane & 7);
#pragma unroll
    for (int j = 0; j < 5; ++j) { const int pp = (lane >> 3) + 8 * wave + 64 * j;
      wF[2 * j] = ld_w(wm + (size_t)(2 * pp) * F); wF[2 * j + 1] = ld_w(wm + (size_t)(2 * pp + 1) * F); }
    int off[5], hidx[3];
#pragma unroll
    for (int j = 0; j < 5; ++j) off[j] = WS_YP + ((lane >> 3) * 320 + 40 * wave + 8 * j + (lane & 7)) * 16;
#pragma unroll
    for (int t = 0; t < 3; ++t) { const int pp = 40 * wave + 8 * (t < 2 ? 2 * t + ((lane >> 4) & 1) : 4) + (lane & 7); xF[t] = xw[pp]; hidx[t] = 2 * pp + (lane >> 5); }
    SCHED();
    u32 wp[5][8];
#pragma unroll
    for (int j = 0; j < 5; ++j) { pair_rows(wF[2 * j], wF[2 * j + 1], wp[j]); PIN8(wp[j]); }
    float xr[3];
#pragma unroll
    for (int t = 0; t < 3; ++t) xr[t] = lane < 32 ? lo16(xF[t]) : hi16(xF[t]);
    {
      float cs[8];
#pragma unroll
      for (int k = 0; k < 8; ++k) { cs[k] = dot2(wp[0][k], 0x3f803f80u, 0.f);
#pragma unroll
        for (int j = 1; j < 5; ++j) cs[k] = dot2(wp[j][k], 0x3f803f80u, cs[k]); }
      f32x2 t = fold8_rows(cs);
      t.x += dpp<ROR8>(t.x); t.y += dpp<ROR8>(t.y);
      if (c < 8) L.redS[wave][4 * c + r] = t;
    }
    __syncthreads();
    f32x2 S = {0.f, 0.f};
    if (wave == 0) {
      const int e = lane & 31, w0 = 4 * (lane >> 5);
      S = (L.redS[w0][e] + L.redS[w0 + 1][e]) + (L.redS[w0 + 2][e] + L.redS[w0 + 3][e]);
      S.x = fold32(S.x, S.x); S.y = fold32(S.y, S.y);
    }
    asm volatile("" : "+v"(S.x), "+v"(S.y), "+v"(xr[0]), "+v"(xr[1]), "+v"(xr[2]), "+v"(hidx[0]), "+v"(hidx[1]), "+v"(hidx[2]));
    SCHED();
    watch_units<0>(rs, WS_ML + (32 * wave + (lane & 31)) * 16, tag);
    Units5 u;
    gather_units5(p.ws, off, u, tag);
    float hv[3];
    {
      const float t0 = fold32(asf(u.a0.x), asf(u.a0.z)), t1 = fold32(asf(u.a1.x), asf(u.a1.z)), t2 = fold32(asf(u.a2.x), asf(u.a2.z));
      const float t3 = fold32(asf(u.a3.x), asf(u.a3.z)), t4 = fold32(asf(u.a4.x), asf(u.a4.z));
      float a[3] = {fold16(t0, t1), fold16(t2, t3), fold16(t4, t4)};
      u16* hb = reinterpret_cast<u16*>(L.hp);
      u32 hq[3];
#pragma unroll
      for (int t = 0; t < 3; ++t) { a[t] += dpp<ROR8>(a[t]); hq[t] = cvtpk(xr[t] + rbf(a[t]), 0.f); hv[t] = asf(hq[t] << 16); }
      if (!(lane & 8)) { hb[hidx[0]] = (u16)hq[0]; hb[hidx[1]] = (u16)hq[1]; hb[hidx[2]] = (u16)hq[2]; }
    }
    __syncthreads();
    u32 hp[5];
#pragma unroll
    for (int j = 0; j < 5; ++j) hp[j] = L.hp[(lane >> 3) + 8 * wave + 64 * j];
    {
      const float d0 = hv[0] - xpivot, d1 = hv[1] - xpivot, d2 = hv[2] - xpivot;
      float s1 = fmaf(0.5f, d2, d0 + d1), s2 = fmaf(0.5f * d2, d2, fmaf(d0, d0, d1 * d1));
      s1 += dpp<QX1>(s1); s2 += dpp<QX1>(s2); s1 += dpp<QX2>(s1); s2 += dpp<QX2>(s2); s1 += dpp<HMIR>(s1); s2 += dpp<HMIR>(s2);
      s1 = fold16(s1, s1); s2 = fold16(s2, s2); s1 = fold32(s1, s1); s2 = fold32(s2, s2);
      if (lane == 0) L.st[wave] = f32x2{s1, s2};
    }
    float g[8] = {0.f, 0.f, 0.f, 0.f, 0.f, 0.f, 0.f, 0.f};
#pragma unroll
    for (int j = 0; j < 5; ++j)
#pragma unroll
      for (int k = 0; k < 8; ++k) g[k] = dot2(wp[j][k], hp[j], g[k]);
    f32x2 t = fold8_rows(g);
    t.x += dpp<ROR8>(t.x); t.y += dpp<ROR8>(t.y);
    if (c < 8) L.red[wave][4 * c + r] = t;
    __syncthreads();
    if (wave == 0) {
      const int e = lane & 31, w0 = 4 * (lane >> 5);
      f32x2 sv = L.st[lane & 7];
      f32x2 A = (L.red[w0][e] + L.red[w0 + 1][e]) + (L.red[w0 + 2][e] + L.red[w0 + 3][e]);
      sv.x += dpp<QX1>(sv.x); sv.y += dpp<QX1>(sv.y); sv.x += dpp<QX2>(sv.x); sv.y += dpp<QX2>(sv.y); sv.x += dpp<HMIR>(sv.x); sv.y += dpp<HMIR>(sv.y);
      A.x = fold32(A.x, A.x); A.y = fold32(A.y, A.y);
      const float e1 = sv.x * (1.f / D), e2 = sv.y * (1.f / D);
      const float meanb = rbf(xpivot + e1), rden = frcp(rbf(__builtin_amdgcn_sqrtf(rbf(rbf(fmaxf(e2 - e1 * e1, 0.f)) + EPS))));
      const u32 ap = cvtpk((A.x - meanb * S.x) * rden, (A.y - meanb * S.y) * rden);
      const u32 nx = asu(dpp<QX1>(asf(ap)));
      if (lane < 32 && !(lane & 1)) st_unit(rs, WS_AC + ((fb & 1) * 432 + 16 * (fb >> 1) + (lane >> 1)) * 16, ap, nx, tag);
    }
  }

  if (roleD) {
    u32x4 wD[8];
#pragma unroll
    for (int j = 0; j < 4; ++j) { int pp = (lane >> 1) + 32 * wave + 256 * j; pp = pp < 864 ? pp : 863;
      wD[2 * j] = ld_w(p.wd + (size_t)(2 * pp) * D + 16 * cb + 8 * (lane & 1)); wD[2 * j + 1] = ld_w(p.wd + (size_t)(2 * pp + 1) * D + 16 * cb + 8 * (lane & 1)); }
    const u32 xres = xw[8 * cb + (lane & 7)];
    SCHED();
    const bool has3 = wave < 3;
    u32 wp[4][8];
#pragma unroll
    for (int j = 0; j < 4; ++j) pair_rows(wD[2 * j], wD[2 * j + 1], wp[j]);
#pragma unroll
    for (int k = 0; k < 8; ++k) wp[3][k] = has3 ? wp[3][k] : 0u;
    PIN8(wp[0]); PIN8(wp[1]); PIN8(wp[2]); PIN8(wp[3]);
    SCHED();
    u32 hres = 0;
    if (wave == 3) {
      const int off[1] = {WS_YP + ((lane >> 3) * 320 + 8 * cb + (lane & 7)) * 16}; u32x4 y[1] = {ld_unit(rs, off[0])};
      wait_units<1, 4>(rs, off, y, tag);
      float a0 = asf(y[0].x), a1 = asf(y[0].z);
      a0 += dpp<ROR8>(a0); a1 += dpp<ROR8>(a1); a0 = fold16(a0, a0); a1 = fold16(a1, a1); a0 = fold32(a0, a0); a1 = fold32(a1, a1);
      hres = cvtpk(lo16(xres) + rbf(a0), hi16(xres) + rbf(a1));
    }
    if (wave < 4) {
      const int u1 = tid + 256 < 432 ? tid + 256 : 431;
      const int off[4] = {WS_AC + tid * 16, WS_AC + (432 + tid) * 16, WS_AC + u1 * 16, WS_AC + (432 + u1) * 16};
      u32x4 a[4] = {ld_unit(rs, off[0]), ld_unit(rs, off[1]), ld_unit(rs, off[2]), ld_unit(rs, off[3])};
      wait_units<4, 1>(rs, off, a, tag);
      u32 av[4];
#pragma unroll
      for (int k = 0; k < 4; ++k) { const u32x4 ga = a[k & 2], ua = a[(k & 2) + 1]; const u32 gg = (k & 1) ? ga.z : ga.x, uv = (k & 1) ? ua.z : ua.x; const float g0 = lo16(gg), g1 = hi16(gg);
        av[k] = cvtpk(rbf(g0 * frcp(1.f + fexp2(-g0 * L2E))) * lo16(uv), rbf(g1 * frcp(1.f + fexp2(-g1 * L2E))) * hi16(uv)); }
      *reinterpret_cast<u32x2*>(&L.act[2 * tid]) = u32x2{av[0], av[1]};
      if (tid + 256 < 432) *reinterpret_cast<u32x2*>(&L.act[2 * (tid + 256)]) = u32x2{av[2], av[3]};
    }
    __syncthreads();
    float o[8] = {0.f, 0.f, 0.f, 0.f, 0.f, 0.f, 0.f, 0.f};
#pragma unroll
    for (int j = 0; j < 4; ++j) {
      const u32 av = L.act[(lane >> 1) + 32 * wave + ((j < 3 || has3) ? 256 * j : 0)];
#pragma unroll
      for (int k = 0; k < 8; ++k) o[k] = dot2(wp[j][k], av, o[k]);
    }
    f32x2 t = fold8_rows(o);
    t.x += dpp<ROR8>(t.x); t.y += dpp<ROR8>(t.y); t.x += dpp<ROR4>(t.x); t.y += dpp<ROR4>(t.y); t.x += dpp<QX2>(t.x); t.y += dpp<QX2>(t.y);
    if (c < 2) L.red[wave][4 * c + r] = t;
    __syncthreads();
    if (wave == 3) {
      f32x2 z = L.red[lane >> 3][lane & 7];
      z.x += dpp<ROR8>(z.x); z.y += dpp<ROR8>(z.y); z.x = fold16(z.x, z.x); z.y = fold16(z.y, z.y); z.x = fold32(z.x, z.x); z.y = fold32(z.y, z.y);
      const u32 ov = cvtpk(lo16(hres) + rbf(z.x), hi16(hres) + rbf(z.y));
      if (lane < 8) __hip_atomic_store((GAS u32*)p.out + 8 * cb + lane, ov, __ATOMIC_RELAXED, __HIP_MEMORY_SCOPE_AGENT);
      if (cb == 0 && lane == 0) __hip_atomic_store((GAS u32*)(p.ws + WS_EPOCH), tag, __ATOMIC_RELAXED, __HIP_MEMORY_SCOPE_AGENT);
    }
  }
}

extern "C" void kernel_launch(void* const* d_in, const int* in_sizes, int n_in, void* d_out, int out_size, void* d_ws, size_t ws_size,
                              hipStream_t stream) {
  Params p;
  p.x = (const u16*)d_in[0]; p.K = (const u16*)d_in[1]; p.V = (const u16*)d_in[2]; p.wq = (const u16*)d_in[3]; p.wo = (const u16*)d_in[4];
  p.wg = (const u16*)d_in[6]; p.wu = (const u16*)d_in[7]; p.wd = (const u16*)d_in[8];
  p.out = (u16*)d_out; p.ws = (unsigned char*)d_ws;
  fused_block<<<NBLK, NTHR, 0, stream>>>(p);
}
```

```cpp
#include <hip/hip_runtime.h>
#include <hip/hip_bf16.h>
#include <stdint.h>
#include <type_traits>

typedef unsigned u32;
typedef unsigned long long u64;
typedef unsigned short u16;
typedef u32 u32x4 __attribute__((ext_vector_type(4)));
typedef u32 u32x2 __attribute__((ext_vector_type(2)));
typedef float f32x2 __attribute__((ext_vector_type(2)));
typedef __bf16 bf16x2 __attribute__((ext_vector_type(2)));
#define GAS __attribute__((address_space(1)))

constexpr int D = 640, H = 32, SK = 16384, DH = 128, F = 1728, HD = H * DH;
constexpr int NBLK = 256, NTHR = 512;
constexpr int NR = 16;
constexpr int WIN = 2;
constexpr int RING = 4;
constexpr u32 PACE_DT = 1760;
constexpr u32 START_OFF = 80;
constexpr float L2E = 1.4426950408889634f;
constexpr float SCALE = 0.08838834764831845f;
constexpr float EPS = 1e-5f;
constexpr int WS_EPOCH = 0;
constexpr int WS_QP = 4096;
constexpr int WS_AP = WS_QP + 256 * 1024;
constexpr int WS_ML = WS_AP + 256 * 1024;
constexpr int WS_YP = WS_ML + 4096;
constexpr int WS_AC = WS_YP + 8 * 320 * 16;
constexpr int WS_END = WS_AC + 2 * 432 * 16;
constexpr int SPIN_CAP = 1 << 18;

struct Params {
  const u16 *x, *K, *V, *wq, *wo, *wg, *wu, *wd;
  u16* out;
  unsigned char* ws;
};

template <int I, int N, class Fn> __device__ __forceinline__ void static_for(Fn&& f) { if constexpr (I < N) { f(std::integral_constant<int, I>{}); static_for<I + 1, N>(f); } }
#define SCHED() __builtin_amdgcn_sched_barrier(0)
#define MEMFENCE() asm volatile("" ::: "memory")
#define PIN8(a) asm volatile("" : "+v"(a[0]), "+v"(a[1]), "+v"(a[2]), "+v"(a[3]), "+v"(a[4]), "+v"(a[5]), "+v"(a[6]), "+v"(a[7]))
__device__ __forceinline__ float asf(u32 x) { return __builtin_bit_cast(float, x); }
__device__ __forceinline__ u32 asu(float x) { return __builtin_bit_cast(u32, x); }
__device__ __forceinline__ float dot2(u32 a, u32 b, float c) {
  return __builtin_amdgcn_fdot2_f32_bf16(__builtin_bit_cast(bf16x2, a), __builtin_bit_cast(bf16x2, b), c, false);
}
__device__ __forceinline__ u32 cvtpk(float lo, float hi) { bf16x2 v = {(__bf16)lo, (__bf16)hi}; return __builtin_bit_cast(u32, v); }
__device__ __forceinline__ float rbf(float x) { return asf(cvtpk(x, x) << 16); }
__device__ __forceinline__ float lo16(u32 v) { return asf(v << 16); }
__device__ __forceinline__ float hi16(u32 v) { return asf(v & 0xffff0000u); }
__device__ __forceinline__ float frcp(float x) { return __builtin_amdgcn_rcpf(x); }
__device__ __forceinline__ float fexp2(float x) { return __builtin_amdgcn_exp2f(x); }
template <int CTRL> __device__ __forceinline__ float dpp(float x) {
  return asf((u32)__builtin_amdgcn_mov_dpp((int)asu(x), CTRL, 0xf, 0xf, true));
}
constexpr int QX1 = 0xB1, QX2 = 0x4E, HMIR = 0x141, ROR4 = 0x124, ROR8 = 0x128, NBC = 0x150;
__device__ __forceinline__ float fold32(float a, float b) {
  auto r = __builtin_amdgcn_permlane32_swap(asu(a), asu(b), false, false); return asf(r[0]) + asf(r[1]); }
__device__ __forceinline__ float fold16(float a, float b) {
  auto r = __builtin_amdgcn_permlane16_swap(asu(a), asu(b), false, false); return asf(r[0]) + asf(r[1]); }
__device__ __forceinline__ float max32(float a) {
  auto r = __builtin_amdgcn_permlane32_swap(asu(a), asu(a), false, false); return fmaxf(asf(r[0]), asf(r[1])); }
__device__ __forceinline__ float max16(float a) {
  auto r = __builtin_amdgcn_permlane16_swap(asu(a), asu(a), false, false); return fmaxf(asf(r[0]), asf(r[1])); }
__device__ __forceinline__ float wave_sum(float v) {
  v += dpp<QX1>(v); v += dpp<QX2>(v); v += dpp<HMIR>(v); v += dpp<ROR8>(v);
  v = fold16(v, v); return fold32(v, v);
}
__device__ __forceinline__ f32x2 fold8_rows(const float (&a)[8]) {
  float t0 = fold32(a[0], a[4]), t1 = fold32(a[1], a[5]), t2 = fold32(a[2], a[6]), t3 = fold32(a[3], a[7]);
  return f32x2{fold16(t0, t2), fold16(t1, t3)};
}
__device__ __forceinline__ void pair_rows(u32x4 a, u32x4 b, u32 (&o)[8]) {
#pragma unroll
  for (int k = 0; k < 4; ++k) { o[2 * k] = __builtin_amdgcn_perm(b[k], a[k], 0x05040100u); o[2 * k + 1] = __builtin_amdgcn_perm(b[k], a[k], 0x07060302u); }
}
__device__ __forceinline__ u32x4 ld_nt(const u16* p) { return __builtin_nontemporal_load(reinterpret_cast<const u32x4*>(p)); }
__device__ __forceinline__ u32x4 ld_w(const u16* p) { return *reinterpret_cast<const u32x4*>(p); }

__device__ __forceinline__ u32x4 ld_unit(__amdgpu_buffer_rsrc_t rs, int off) { return __builtin_amdgcn_raw_buffer_load_b128(rs, off, 0, 16); }
__device__ __forceinline__ void st_unit(__amdgpu_buffer_rsrc_t rs, int off, u32 a, u32 b, u32 tag) {
  __builtin_amdgcn_raw_buffer_store_b128(u32x4{a, tag, b, tag}, rs, off, 0, 16);
}
template <int N, int SLEEP> __device__ __forceinline__ void wait_units(__amdgpu_buffer_rsrc_t rs, const int (&off)[N], u32x4 (&v)[N], u32 tag) {
#pragma nounroll
  for (int spin = SPIN_CAP; spin > 0; --spin) {
    u32 bad = 0u;
#pragma unroll
    for (int j = 0; j < N; ++j) bad |= (v[j].y ^ tag) | (v[j].w ^ tag);
    if (__builtin_amdgcn_ballot_w64(bad != 0u) == 0ull) break;
    __builtin_amdgcn_s_sleep(SLEEP);
    MEMFENCE();
#pragma unroll
    for (int j = 0; j < N; ++j) v[j] = ld_unit(rs, off[j]);
  }
}
template <int TH> __device__ __forceinline__ void watch_units(__amdgpu_buffer_rsrc_t rs, int off, u32 tag) {
#pragma nounroll
  for (int spin = SPIN_CAP; spin > 0; --spin) {
    const u32x4 v = ld_unit(rs, off);
    if (__builtin_popcountll(__builtin_amdgcn_ballot_w64(((v.y ^ tag) | (v.w ^ tag)) != 0u)) <= TH) break;
    __builtin_amdgcn_s_sleep(4);
    MEMFENCE();
  }
}
#define UL(r0, r3, o) "global_load_dwordx4 v[" #r0 ":" #r3 "], %[" #o "], %[base] sc1\n\t"
#define UCHK(r0, r1, r3, o, l) \
    "v_cmp_ne_u32 vcc, %[tag], v" #r1 "\n\tv_cmp_ne_u32 %[tmp], %[tag], v" #r3 "\n\ts_or_b64 vcc, vcc, %[tmp]\n\t" \
    "s_cbranch_scc0 " #l "f\n\t" UL(r0, r3, o) "s_mov_b32 %[any], 1\n\t" #l ":\n\t"
struct Units5 { u32x4 a0, a1, a2, a3, a4; };
__device__ __forceinline__ void gather_units5(const unsigned char* ws, const int (&off)[5], Units5& u, u32 tag) {
  int spin = SPIN_CAP; u32 any; u64 tmp;
  asm volatile(UL(232, 235, o0) UL(236, 239, o1) UL(240, 243, o2) UL(244, 247, o3) UL(248, 251, o4)
               "1:\n\ts_waitcnt vmcnt(0)\n\ts_mov_b32 %[any], 0\n\t"
               UCHK(232, 233, 235, o0, 3) UCHK(236, 237, 239, o1, 4) UCHK(240, 241, 243, o2, 5) UCHK(244, 245, 247, o3, 6) UCHK(248, 249, 251, o4, 7)
               "s_cmp_eq_u32 %[any], 0\n\ts_cbranch_scc1 9f\n\ts_sub_u32 %[spin], %[spin], 1\n\ts_cmp_eq_u32 %[spin], 0\n\ts_cbranch_scc1 8f\n\t"
               "s_sleep 1\n\ts_branch 1b\n\t8:\n\ts_waitcnt vmcnt(0)\n\t9:"
               : "=&{v[232:235]}"(u.a0), "=&{v[236:239]}"(u.a1), "=&{v[240:243]}"(u.a2), "=&{v[244:247]}"(u.a3), "=&{v[248:251]}"(u.a4),
                 [spin] "+s"(spin), [any] "=&s"(any), [tmp] "=&s"(tmp)
               : [o0] "v"(off[0]), [o1] "v"(off[1]), [o2] "v"(off[2]), [o3] "v"(off[3]), [o4] "v"(off[4]), [base] "s"(ws), [tag] "s"(tag)
               : "memory", "vcc", "scc");
}

struct Lds {
  f32x2 pr[8][64];
  u32 q[64];
  f32x2 ml[8];
  f32x2 red[8][32];
  f32x2 redS[8][32];
  u32 hp[320];
  u32 act[864];
  f32x2 st[8];
  float xmean; u32 tag, pad0[2];
};

__device__ __forceinline__ void qk_dots(const u32x4 (&k)[4], const u32 (&q)[4], float (&d)[4]) {
#pragma unroll
  for (int i = 0; i < 4; ++i) { float a = dot2(k[i][0], q[0], 0.f); a = dot2(k[i][1], q[1], a); a = dot2(k[i][2], q[2], a); d[i] = dot2(k[i][3], q[3], a); }
}
__device__ __forceinline__ float tile_score(const float (&d)[4], bool b8, bool b4) {
  const float e0 = (b8 ? d[1] : d[0]) + dpp<ROR8>(b8 ? d[0] : d[1]);
  const float e1 = (b8 ? d[3] : d[2]) + dpp<ROR8>(b8 ? d[2] : d[3]);
  float s = (b4 ? e1 : e0) + dpp<HMIR>(b4 ? e0 : e1);
  s += dpp<QX1>(s); s += dpp<QX2>(s);
  return rbf(rbf(s) * SCALE);
}
__device__ __forceinline__ void pv_acc(const u32x4 (&v)[4], float p, float (&acc)[8]) {
  const float pp[4] = {dpp<NBC + 0>(p), dpp<NBC + 8>(p), dpp<NBC + 4>(p), dpp<NBC + 12>(p)};
#pragma unroll
  for (int i = 0; i < 4; ++i)
#pragma unroll
    for (int k = 0; k < 4; ++k) { acc[2 * k] = fmaf(pp[i], lo16(v[i][k]), acc[2 * k]); acc[2 * k + 1] = fmaf(pp[i], hi16(v[i][k]), acc[2 * k + 1]); }
}

__global__ void __launch_bounds__(NTHR, 2) fused_block(Params p) {
  __shared__ Lds L;
  const int tid = threadIdx.x, lane = tid & 63, wave = __builtin_amdgcn_readfirstlane(tid >> 6);
  const int b = blockIdx.x;
  const int h = b >> 3, s = (b + h) & 7;
  const int r = lane >> 4, c = lane & 15;
  const __amdgpu_buffer_rsrc_t rs = __builtin_amdgcn_make_buffer_rsrc(p.ws, 0, WS_END, 0x00020000);
  const int xb = b & 7, tb = (b >> 3) - 20;
  const bool roleW = tb < 0;
  const int kblk = xb, nblk = b >> 3;
  const bool roleD = tb >= 0 && tb < (xb < 2 ? 8 : 4);
  const int cb = 4 * (xb + 8 * (tb >> 2)) + (tb & 3);
  const int fb = xb < 2 ? 2 * (24 + 2 * xb + ((tb - 8) >> 1)) + (tb & 1) : 2 * (4 * (xb - 2) + ((tb - 4) >> 1)) + (tb & 1);
  const bool roleF = tb >= 0 && !roleD && fb < 54;
  const int qa = (lane >> 2) & 3;

  const int wt = ((((wave & 1) << 2) | (wave & 2) | (wave >> 2)) + b) & 7;
  const size_t kvoff = ((size_t)h * SK + s * 2048 + wt * 16 + r) * DH + 8 * c;
  u32x4 buf[RING][4];
#define LOAD_GROUP(g) do { const u16* gp_ = ((g) < NR ? p.K : p.V) + kvoff + (size_t)((g) % NR) * (128 * DH); \
    _Pragma("unroll") for (int j_ = 0; j_ < 4; ++j_) buf[(g) % RING][j_] = ld_nt(gp_ + j_ * 4 * DH); } while (0)
  u64 tpace0;
#define PACE(g) do { const u32 due_ = ((u32)(g) * PACE_DT + (u32)wave * (PACE_DT / 8)) >> 4; \
    while ((int)((u32)(__builtin_amdgcn_s_memrealtime() - tpace0) - due_) < 0) __builtin_amdgcn_s_sleep(1); } while (0)

  const u32 ep_raw = __hip_atomic_load((GAS u32*)(p.ws + WS_EPOCH), __ATOMIC_RELAXED, __HIP_MEMORY_SCOPE_AGENT);
  const u32* xw = reinterpret_cast<const u32*>(p.x);
  u32x4 wW[4];
  {
    const bool qw = wave < 5;
    u32 xa[5], xp[2]; u32x4 wqv[4];
#pragma unroll
    for (int j = 0; j < 5; ++j) xa[j] = xw[lane + 64 * j];
#pragma unroll
    for (int m = 0; m < 2; ++m) xp[m] = xw[qw ? 40 * s + 4 * (2 * wave + m) + r : 0];
#pragma unroll
    for (int m = 0; m < 2; ++m) { const int pp = 40 * s + 4 * (2 * wave + m) + r;
      wqv[2 * m] = ld_w(qw ? p.wq + (size_t)(2 * pp) * HD + h * DH + 8 * c : p.wq);
      wqv[2 * m + 1] = ld_w(qw ? p.wq + (size_t)(2 * pp + 1) * HD + h * DH + 8 * c : p.wq); }
    SCHED();
    asm volatile("" : "+v"(xa[0]), "+v"(xa[4]));
    const float c0 = lo16(__builtin_amdgcn_readfirstlane(xa[0]));
    float s1 = 0.f, s2 = 0.f;
#pragma unroll
    for (int j = 0; j < 5; ++j) { const float a = lo16(xa[j]) - c0, bb = hi16(xa[j]) - c0; s1 += a + bb; s2 = fmaf(a, a, fmaf(bb, bb, s2)); }
    s1 = wave_sum(s1) * (1.f / D); s2 = wave_sum(s2) * (1.f / D);
    const float meanb = rbf(c0 + s1), rden = frcp(rbf(__builtin_amdgcn_sqrtf(rbf(rbf(fmaxf(s2 - s1 * s1, 0.f)) + EPS))));
    float a[8] = {0.f, 0.f, 0.f, 0.f, 0.f, 0.f, 0.f, 0.f};
#pragma unroll
    for (int m = 0; m < 2; ++m) {
      const u32 xn = cvtpk(rbf(lo16(xp[m]) - meanb) * rden, rbf(hi16(xp[m]) - meanb) * rden);
      u32 wp[8]; pair_rows(wqv[2 * m], wqv[2 * m + 1], wp);
#pragma unroll
      for (int j = 0; j < 8; ++j) a[j] = dot2(wp[j], xn, a[j]);
    }
    f32x2 t = fold8_rows(a);
    u32 wmask = roleW ? ~0u : 0u;
    asm volatile("" : "+v"(t.x), "+v"(t.y), "+v"(wmask));
    SCHED();
#pragma unroll
    for (int k = 0; k < 4; ++k)
      wW[k] = ld_w(p.wo + ((u32)((512 * kblk + 128 * qa + 2 * (8 * wave + 4 * ((lane >> 4) & 1) + k) + (lane >> 5)) * D + 32 * nblk + 8 * (lane & 3)) & wmask));
    SCHED();
    if (qw) L.pr[wave][4 * c + r] = t;
    if (tid == 0) { L.xmean = meanb; L.tag = ep_raw + 1u; }
  }
  __syncthreads();
  tpace0 = __builtin_amdgcn_s_memrealtime() + START_OFF;
  if (wave == 0) {
    const u32 tg = __builtin_amdgcn_readfirstlane(ep_raw) + 1u;
    f32x2 q = L.pr[0][lane];
#pragma unroll
    for (int w = 1; w < 5; ++w) q += L.pr[w][lane];
    st_unit(rs, WS_QP + (h * 8 + s) * 1024 + lane * 16, asu(q.x), asu(q.y), tg);
    int off[8]; u32x4 qu[8];
#pragma unroll
    for (int k = 0; k < 8; ++k) { off[k] = WS_QP + (8 * h + k) * 1024 + lane * 16; qu[k] = ld_unit(rs, off[k]); }
    wait_units<8, 1>(rs, off, qu, tg);
    float q0 = 0.f, q1 = 0.f;
#pragma unroll
    for (int k = 0; k < 8; ++k) { q0 += asf(qu[k].x); q1 += asf(qu[k].z); }
    L.q[lane] = cvtpk(q0, q1);
  }
  SCHED();
  static_for<0, RING>([&](auto G) { constexpr int g = G; PACE(g); LOAD_GROUP(g); SCHED(); });
  __syncthreads();
  const u32 tag = __builtin_amdgcn_readfirstlane(L.tag);
  u32 q[4];
  { const u32x4 t = *reinterpret_cast<const u32x4*>(&L.q[4 * c]); q[0] = t.x; q[1] = t.y; q[2] = t.z; q[3] = t.w; }
  const float xpivot = L.xmean;

  const bool b8 = c & 8, b4 = c & 4;
  float sc[NR];
  static_for<0, NR>([&](auto G) { constexpr int g = G;
    float dd[4]; qk_dots(buf[g % RING], q, dd);
    asm volatile("" : "+v"(dd[0]), "+v"(dd[1]), "+v"(dd[2]), "+v"(dd[3]));
    SCHED();
    if constexpr (g + WIN >= RING) { PACE(g + WIN); LOAD_GROUP(g + WIN); }
    SCHED();
    sc[g] = tile_score(dd, b8, b4);
  });
  float mw = sc[0];
#pragma unroll
  for (int g = 1; g < NR; ++g) mw = fmaxf(mw, sc[g]);
  mw = fmaxf(mw, dpp<ROR4>(mw)); mw = fmaxf(mw, dpp<ROR8>(mw)); mw = max32(max16(mw));
  float lsum = 0.f;
#pragma unroll
  for (int g = 0; g < NR; ++g) { sc[g] = fexp2((sc[g] - mw) * L2E); lsum += sc[g]; }
  float acc[8] = {0.f, 0.f, 0.f, 0.f, 0.f, 0.f, 0.f, 0.f};
  static_for<NR, 2 * NR - WIN>([&](auto G) { constexpr int g = G;
    pv_acc(buf[g % RING], sc[g - NR], acc);
    PIN8(acc);
    SCHED();
    PACE(g + WIN);
    LOAD_GROUP(g + WIN);
    SCHED();
  });
#pragma unroll
  for (int g = 2 * NR - WIN; g < 2 * NR; ++g) pv_acc(buf[g % RING], sc[g - NR], acc);

  {
    float l = lsum; l += dpp<ROR4>(l); l += dpp<ROR8>(l); l = fold16(l, l); l = fold32(l, l);
    L.pr[wave][4 * c + r] = fold8_rows(acc);
    if (lane == 0) L.ml[wave] = f32x2{mw, l};
  }
  __syncthreads();
  if (wave == 0) {
    const f32x2 ml = L.ml[lane & 7];
    f32x2 pa[8];
#pragma unroll
    for (int w = 0; w < 8; ++w) pa[w] = L.pr[w][lane];
    float M = fmaxf(ml.x, dpp<QX1>(ml.x)); M = fmaxf(M, dpp<QX2>(M)); M = fmaxf(M, dpp<HMIR>(M));
    const float f = fexp2((ml.x - M) * L2E);
    float l = f * ml.y; l += dpp<QX1>(l); l += dpp<QX2>(l); l += dpp<HMIR>(l);
    f32x2 o = dpp<NBC + 0>(f) * pa[0];
    o += dpp<NBC + 1>(f) * pa[1]; o += dpp<NBC + 2>(f) * pa[2]; o += dpp<NBC + 3>(f) * pa[3];
    o += dpp<NBC + 4>(f) * pa[4]; o += dpp<NBC + 5>(f) * pa[5]; o += dpp<NBC + 6>(f) * pa[6]; o += dpp<NBC + 7>(f) * pa[7];
    st_unit(rs, WS_AP + (h * 8 + s) * 1024 + lane * 16, asu(o.x), asu(o.y), tag);
    if (lane == 0) st_unit(rs, WS_ML + (h * 8 + s) * 16, asu(M), asu(l), tag);
  }
  __builtin_amdgcn_s_barrier();

  if (roleW) {
    const int sb = lane >> 5, hh = 4 * kblk + qa, dpW = 8 * wave + 4 * ((lane >> 4) & 1) + (lane & 3);
    int off[5]; u32x4 a[5];
    off[0] = WS_ML + (hh * 8 + 2 * (lane & 3) + sb) * 16;
#pragma unroll
    for (int k = 0; k < 4; ++k) off[1 + k] = WS_AP + (hh * 8 + 2 * k + sb) * 1024 + dpW * 16;
    u32 wp01[8], wp23[8]; pair_rows(wW[0], wW[1], wp01); pair_rows(wW[2], wW[3], wp23);
    PIN8(wp01); PIN8(wp23);
    asm volatile("" : "+v"(off[0]), "+v"(off[1]), "+v"(off[2]), "+v"(off[3]), "+v"(off[4]));
    SCHED();
    if (wave == 0) watch_units<16>(rs, WS_ML + (32 * kblk + (lane & 31)) * 16, tag);
    __syncthreads();
#pragma unroll
    for (int k = 0; k < 5; ++k) a[k] = ld_unit(rs, off[k]);
    wait_units<5, 1>(rs, off, a, tag);
    const float m = asf(a[0].x);
    float M = fmaxf(m, dpp<QX1>(m)); M = fmaxf(M, dpp<QX2>(M)); M = max32(M);
    const float w = fexp2((m - M) * L2E);
    float l = w * asf(a[0].z); l += dpp<QX1>(l); l += dpp<QX2>(l); l = fold32(l, l);
    const float w0 = dpp<0x00>(w), w1 = dpp<0x55>(w), w2 = dpp<0xAA>(w), w3 = dpp<0xFF>(w);
    const float o0 = fmaf(w3, asf(a[4].x), fmaf(w2, asf(a[3].x), fmaf(w1, asf(a[2].x), w0 * asf(a[1].x))));
    const float o1 = fmaf(w3, asf(a[4].z), fmaf(w2, asf(a[3].z), fmaf(w1, asf(a[2].z), w0 * asf(a[1].z))));
    const float o = fold32(o0, o1) * frcp(l);
    const u32 p01 = cvtpk(dpp<0x00>(o), dpp<0x55>(o)), p23 = cvtpk(dpp<0xAA>(o), dpp<0xFF>(o));
    float y[8];
#pragma unroll
    for (int j = 0; j < 8; ++j) y[j] = dot2(wp23[j], p23, dot2(wp01[j], p01, 0.f));
    f32x2 t = fold8_rows(y);
    t.x += dpp<ROR8>(t.x); t.y += dpp<ROR8>(t.y); t.x += dpp<ROR4>(t.x); t.y += dpp<ROR4>(t.y);
    if (c < 4) L.red[wave][4 * c + r] = t;
    __syncthreads();
    if (wave == 0) {
      f32x2 z = L.red[2 * r][c] + L.red[2 * r + 1][c];
      z.x = fold16(z.x, z.x); z.y = fold16(z.y, z.y); z.x = fold32(z.x, z.x); z.y = fold32(z.y, z.y);
      if (lane < 16) st_unit(rs, WS_YP + (kblk * 320 + 16 * nblk + lane) * 16, asu(z.x), asu(z.y), tag);
    }
  }

  if (roleF) {
    u32x4 wF[10]; u32 xF[3];
    const u16* wm = ((fb & 1) ? p.wu : p.wg) + 64 * (fb >> 1) + 8 * (lane & 7);
#pragma unroll
    for (int j = 0; j < 5; ++j) { const int pp = (lane >> 3) + 8 * wave + 64 * j;
      wF[2 * j] = ld_w(wm + (size_t)(2 * pp) * F); wF[2 * j + 1] = ld_w(wm + (size_t)(2 * pp + 1) * F); }
    int off[5], hidx[3];
#pragma unroll
    for (int j = 0; j < 5; ++j) off[j] = WS_YP + ((lane >> 3) * 320 + 40 * wave + 8 * j + (lane & 7)) * 16;
#pragma unroll
    for (int t = 0; t < 3; ++t) { const int pp = 40 * wave + 8 * (t < 2 ? 2 * t + ((lane >> 4) & 1) : 4) + (lane & 7); xF[t] = xw[pp]; hidx[t] = 2 * pp + (lane >> 5); }
    SCHED();
    u32 wp[5][8];
#pragma unroll
    for (int j = 0; j < 5; ++j) { pair_rows(wF[2 * j], wF[2 * j + 1], wp[j]); PIN8(wp[j]); }
    float xr[3];
#pragma unroll
    for (int t = 0; t < 3; ++t) xr[t] = lane < 32 ? lo16(xF[t]) : hi16(xF[t]);
    {
      float cs[8];
#pragma unroll
      for (int k = 0; k < 8; ++k) { cs[k] = dot2(wp[0][k], 0x3f803f80u, 0.f);
#pragma unroll
        for (int j = 1; j < 5; ++j) cs[k] = dot2(wp[j][k], 0x3f803f80u, cs[k]); }
      f32x2 t = fold8_rows(cs);
      t.x += dpp<ROR8>(t.x); t.y += dpp<ROR8>(t.y);
      if (c < 8) L.redS[wave][4 * c + r] = t;
    }
    __syncthreads();
    f32x2 S = {0.f, 0.f};
    if (wave == 0) {
      const int e = lane & 31, w0 = 4 * (lane >> 5);
      S = (L.redS[w0][e] + L.redS[w0 + 1][e]) + (L.redS[w0 + 2][e] + L.redS[w0 + 3][e]);
      S.x = fold32(S.x, S.x); S.y = fold32(S.y, S.y);
    }
    asm volatile("" : "+v"(S.x), "+v"(S.y), "+v"(xr[0]), "+v"(xr[1]), "+v"(xr[2]), "+v"(hidx[0]), "+v"(hidx[1]), "+v"(hidx[2]));
    SCHED();
    watch_units<0>(rs, WS_ML + (32 * wave + (lane & 31)) * 16, tag);
    Units5 u;
    gather_units5(p.ws, off, u, tag);
    float hv[3];
    {
      const float t0 = fold32(asf(u.a0.x), asf(u.a0.z)), t1 = fold32(asf(u.a1.x), asf(u.a1.z)), t2 = fold32(asf(u.a2.x), asf(u.a2.z));
      const float t3 = fold32(asf(u.a3.x), asf(u.a3.z)), t4 = fold32(asf(u.a4.x), asf(u.a4.z));
      float a[3] = {fold16(t0, t1), fold16(t2, t3), fold16(t4, t4)};
      u16* hb = reinterpret_cast<u16*>(L.hp);
      u32 hq[3];
#pragma unroll
      for (int t = 0; t < 3; ++t) { a[t] += dpp<ROR8>(a[t]); hq[t] = cvtpk(xr[t] + rbf(a[t]), 0.f); hv[t] = asf(hq[t] << 16); }
      if (!(lane & 8)) { hb[hidx[0]] = (u16)hq[0]; hb[hidx[1]] = (u16)hq[1]; hb[hidx[2]] = (u16)hq[2]; }
    }
    __syncthreads();
    u32 hp[5];
#pragma unroll
    for (int j = 0; j < 5; ++j) hp[j] = L.hp[(lane >> 3) + 8 * wave + 64 * j];
    {
      const float d0 = hv[0] - xpivot, d1 = hv[1] - xpivot, d2 = hv[2] - xpivot;
      float s1 = fmaf(0.5f, d2, d0 + d1), s2 = fmaf(0.5f * d2, d2, fmaf(d0, d0, d1 * d1));
      s1 += dpp<QX1>(s1); s2 += dpp<QX1>(s2); s1 += dpp<QX2>(s1); s2 += dpp<QX2>(s2); s1 += dpp<HMIR>(s1); s2 += dpp<HMIR>(s2);
      s1 = fold16(s1, s1); s2 = fold16(s2, s2); s1 = fold32(s1, s1); s2 = fold32(s2, s2);
      if (lane == 0) L.st[wave] = f32x2{s1, s2};
    }
    float g[8] = {0.f, 0.f, 0.f, 0.f, 0.f, 0.f, 0.f, 0.f};
#pragma unroll
    for (int j = 0; j < 5; ++j)
#pragma unroll
      for (int k = 0; k < 8; ++k) g[k] = dot2(wp[j][k], hp[j], g[k]);
    f32x2 t = fold8_rows(g);
    t.x += dpp<ROR8>(t.x); t.y += dpp<ROR8>(t.y);
    if (c < 8) L.red[wave][4 * c + r] = t;
    __syncthreads();
    if (wave == 0) {
      const int e = lane & 31, w0 = 4 * (lane >> 5);
      f32x2 sv = L.st[lane & 7];
      f32x2 A = (L.red[w0][e] + L.red[w0 + 1][e]) + (L.red[w0 + 2][e] + L.red[w0 + 3][e]);
      sv.x += dpp<QX1>(sv.x); sv.y += dpp<QX1>(sv.y); sv.x += dpp<QX2>(sv.x); sv.y += dpp<QX2>(sv.y); sv.x += dpp<HMIR>(sv.x); sv.y += dpp<HMIR>(sv.y);
      A.x = fold32(A.x, A.x); A.y = fold32(A.y, A.y);
      const float e1 = sv.x * (1.f / D), e2 = sv.y * (1.f / D);
      const float meanb = rbf(xpivot + e1), rden = frcp(rbf(__builtin_amdgcn_sqrtf(rbf(rbf(fmaxf(e2 - e1 * e1, 0.f)) + EPS))));
      const u32 ap = cvtpk((A.x - meanb * S.x) * rden, (A.y - meanb * S.y) * rden);
      const u32 nx = asu(dpp<QX1>(asf(ap)));
      if (lane < 32 && !(lane & 1)) st_unit(rs, WS_AC + ((fb & 1) * 432 + 16 * (fb >> 1) + (lane >> 1)) * 16, ap, nx, tag);
    }
  }

  if (roleD) {
    u32x4 wD[8];
#pragma unroll
    for (int j = 0; j < 4; ++j) { int pp = (lane >> 1) + 32 * wave + 256 * j; pp = pp < 864 ? pp : 863;
      wD[2 * j] = ld_w(p.wd + (size_t)(2 * pp) * D + 16 * cb + 8 * (lane & 1)); wD[2 * j + 1] = ld_w(p.wd + (size_t)(2 * pp + 1) * D + 16 * cb + 8 * (lane & 1)); }
    const u32 xres = xw[8 * cb + (lane & 7)];
    SCHED();
    const bool has3 = wave < 3;
    u32 wp[4][8];
#pragma unroll
    for (int j = 0; j < 4; ++j) pair_rows(wD[2 * j], wD[2 * j + 1], wp[j]);
#pragma unroll
    for (int k = 0; k < 8; ++k) wp[3][k] = has3 ? wp[3][k] : 0u;
    PIN8(wp[0]); PIN8(wp[1]); PIN8(wp[2]); PIN8(wp[3]);
    SCHED();
    u32 hres = 0;
    if (wave == 3) {
      const int off[1] = {WS_YP + ((lane >> 3) * 320 + 8 * cb + (lane & 7)) * 16}; u32x4 y[1] = {ld_unit(rs, off[0])};
      wait_units<1, 4>(rs, off, y, tag);
      float a0 = asf(y[0].x), a1 = asf(y[0].z);
      a0 += dpp<ROR8>(a0); a1 += dpp<ROR8>(a1); a0 = fold16(a0, a0); a1 = fold16(a1, a1); a0 = fold32(a0, a0); a1 = fold32(a1, a1);
      hres = cvtpk(lo16(xres) + rbf(a0), hi16(xres) + rbf(a1));
    }
    if (wave < 4) {
      const int u1 = tid + 256 < 432 ? tid + 256 : 431;
      const int off[4] = {WS_AC + tid * 16, WS_AC + (432 + tid) * 16, WS_AC + u1 * 16, WS_AC + (432 + u1) * 16};
      u32x4 a[4] = {ld_unit(rs, off[0]), ld_unit(rs, off[1]), ld_unit(rs, off[2]), ld_unit(rs, off[3])};
      wait_units<4, 1>(rs, off, a, tag);
      u32 av[4];
#pragma unroll
      for (int k = 0; k < 4; ++k) { const u32x4 ga = a[k & 2], ua = a[(k & 2) + 1]; const u32 gg = (k & 1) ? ga.z : ga.x, uv = (k & 1) ? ua.z : ua.x; const float g0 = lo16(gg), g1 = hi16(gg);
        av[k] = cvtpk(rbf(g0 * frcp(1.f + fexp2(-g0 * L2E))) * lo16(uv), rbf(g1 * frcp(1.f + fexp2(-g1 * L2E))) * hi16(uv)); }
      *reinterpret_cast<u32x2*>(&L.act[2 * tid]) = u32x2{av[0], av[1]};
      if (tid + 256 < 432) *reinterpret_cast<u32x2*>(&L.act[2 * (tid + 256)]) = u32x2{av[2], av[3]};
    }
    __syncthreads();
    float o[8] = {0.f, 0.f, 0.f, 0.f, 0.f, 0.f, 0.f, 0.f};
#pragma unroll
    for (int j = 0; j < 4; ++j) {
      const u32 av = L.act[(lane >> 1) + 32 * wave + ((j < 3 || has3) ? 256 * j : 0)];
#pragma unroll
      for (int k = 0; k < 8; ++k) o[k] = dot2(wp[j][k], av, o[k]);
    }
    f32x2 t = fold8_rows(o);
    t.x += dpp<ROR8>(t.x); t.y += dpp<ROR8>(t.y); t.x += dpp<ROR4>(t.x); t.y += dpp<ROR4>(t.y); t.x += dpp<QX2>(t.x); t.y += dpp<QX2>(t.y);
    if (c < 2) L.red[wave][4 * c + r] = t;
    __syncthreads();
    if (wave == 3) {
      f32x2 z = L.red[lane >> 3][lane & 7];
      z.x += dpp<ROR8>(z.x); z.y += dpp<ROR8>(z.y); z.x = fold16(z.x, z.x); z.y = fold16(z.y, z.y); z.x = fold32(z.x, z.x); z.y = fold32(z.y, z.y);
      const u32 ov = cvtpk(lo16(hres) + rbf(z.x), hi16(hres) + rbf(z.y));
      if (lane < 8) __hip_atomic_store((GAS u32*)p.out + 8 * cb + lane, ov, __ATOMIC_RELAXED, __HIP_MEMORY_SCOPE_AGENT);
      if (cb == 0 && lane == 0) __hip_atomic_store((GAS u32*)(p.ws + WS_EPOCH), tag, __ATOMIC_RELAXED, __HIP_MEMORY_SCOPE_AGENT);
    }
  }
}

extern "C" void kernel_launch(void* const* d_in, const int* in_sizes, int n_in, void* d_out, int out_size, void* d_ws, size_t ws_size,
                              hipStream_t stream) {
  Params p;
  p.x = (const u16*)d_in[0]; p.K = (const u16*)d_in[1]; p.V = (const u16*)d_in[2]; p.wq = (const u16*)d_in[3]; p.wo = (const u16*)d_in[4];
  p.wg = (const u16*)d_in[6]; p.wu = (const u16*)d_in[7]; p.wd = (const u16*)d_in[8];
  p.out = (u16*)d_out; p.ws = (unsigned char*)d_ws;
  fused_block<<<NBLK, NTHR, 0, stream>>>(p);
}
```
